```python
import torch

def _softmax(x, axis=-1):
    return torch.softmax(x, dim=axis)
def _gelu(x):
    return torch.nn.functional.gelu(x, approximate="tanh")
def _silu(x):
    return torch.nn.functional.silu(x)
def _sigmoid(x):
    return torch.sigmoid(x)
def _layernorm(x, axis=-1, eps=1e-5):
    m = x.mean(dim=axis, keepdim=True)
    v = x.var(dim=axis, keepdim=True, unbiased=False)
    return (x - m) / torch.sqrt(v + eps)
def _scatter_add(tgt, idx, src, axis=0):
    return tgt.clone().index_add_(axis, idx, src)
def _segment_sum(src, seg, n):
    out = torch.zeros((n,) + src.shape[1:], dtype=src.dtype)
    return out.index_add_(0, seg, src)
def _pad(x, pw):
    flat = tuple(v for pair in reversed(pw) for v in pair)
    return torch.nn.functional.pad(x, flat)

kernel_name = 'standalone__gqa_softmax__near_ridge'

def setup_inputs():

    _w = (lambda a: (lambda t: t.to(torch.bfloat16) if t.is_floating_point() else t)(torch.as_tensor(a)))
    B, Hq, Hkv, Sq, Sk, D = 8, 32, 16, 336, 1024, 128
    return {
        "q": _w((lambda *s: torch.randn(*s, dtype=torch.bfloat16) * 0.8)(B, Hq, Sq, D)),
        "k": _w((lambda *s: torch.randn(*s, dtype=torch.bfloat16) * 0.8)(B, Hkv, Sk, D)),
        "v": _w((lambda *s: torch.randn(*s, dtype=torch.bfloat16) * 0.05)(B, Hkv, Sk, D)),
    }

def reference(q, k, v):
    B, Hq, Sq, D = q.shape
    Hkv = k.shape[1]
    rep = Hq // Hkv
    k = (lambda x, n, axis: torch.repeat_interleave(x, n, dim=axis))(k, rep, axis=1)
    v = (lambda x, n, axis: torch.repeat_interleave(x, n, dim=axis))(v, rep, axis=1)
    scale = D ** -0.5
    scores = torch.einsum("bhqd,bhkd->bhqk", q, k) * scale
    p = _softmax(scores, axis=-1)
    return torch.einsum("bhqk,bhkd->bhqd", p, v)
```

```cpp
#include <hip/hip_runtime.h>
#include <hip/hip_bf16.h>
using bf16 = __hip_bfloat16;
constexpr int D = 128, NW = 8, QBLK = 32, KVBLK = 64;
constexpr float SCALE = 0.088388347648318440f;
constexpr float THR = 8.f;
constexpr int B = 8, HQ = 32, HKV = 16, SQ = 336, SKV = 1024, ROWS = 672;
constexpr size_t SHM_V = KVBLK * D * 2, SHM_K = KVBLK * D * 2, SHM_ATTN = 2 * SHM_V + 2 * SHM_K + NW * 64 * 4;
using bf16x8 = __attribute__((ext_vector_type(8))) short;
using s16x4  = __attribute__((ext_vector_type(4))) short;
using f32x16 = __attribute__((ext_vector_type(16))) float;
using u32x4  = __attribute__((ext_vector_type(4))) unsigned;
#define KSWZ(row, colB) ((row) * 256 + ((colB) ^ (((row) & 7) << 4)))
#define SBAR() __builtin_amdgcn_sched_barrier(0)
__device__ __forceinline__ int crow(int r, int hi) { return (r & 3) + 8 * (r >> 2) + 4 * hi; }
__device__ __forceinline__ unsigned cvtpk(float lo, float hi) {
  unsigned r; asm volatile("v_cvt_pk_bf16_f32 %0, %1, %2" : "=v"(r) : "v"(lo), "v"(hi)); return r;
}
__device__ __forceinline__ bf16x8 ld8(const bf16* p) { return *reinterpret_cast<const bf16x8*>(p); }
__device__ __forceinline__ void partialSM(f32x16& p0, f32x16& p1, float& m_reg, float& mn, float& alpha) {
  constexpr float C = SCALE * 1.4426950408889634f;
  float pmax = p0[0]; for (int r = 1; r < 16; ++r) pmax = fmaxf(pmax, p0[r]); for (int r = 0; r < 16; ++r) pmax = fmaxf(pmax, p1[r]);
  { auto rr = __builtin_amdgcn_permlane32_swap(__float_as_uint(pmax), __float_as_uint(pmax), false, false);
    pmax = fmaxf(__uint_as_float(rr[0]), __uint_as_float(rr[1])); }
  if (__builtin_expect(__all(pmax - m_reg <= THR / SCALE), 1)) { mn = m_reg; alpha = 1.f; }
  else { mn = fmaxf(m_reg, pmax); alpha = __builtin_amdgcn_exp2f((m_reg - mn) * C); m_reg = mn; }
  float mnC = -mn * C;
  for (int r = 0; r < 16; ++r) p0[r] = fmaf(p0[r], C, mnC); for (int r = 0; r < 16; ++r) p1[r] = fmaf(p1[r], C, mnC);
  for (int r = 0; r < 16; ++r) p0[r] = __builtin_amdgcn_exp2f(p0[r]);
}
__device__ __forceinline__ void finishSM(f32x16& p0, f32x16& p1, float alpha, float& l_reg, bf16x8& pa0, bf16x8& pa1, bf16x8& pa2, bf16x8& pa3) {
  for (int r = 0; r < 16; ++r) p1[r] = __builtin_amdgcn_exp2f(p1[r]);
  float ps = 0; for (int r = 0; r < 16; ++r) ps += p0[r]; for (int r = 0; r < 16; ++r) ps += p1[r];
  { auto rr = __builtin_amdgcn_permlane32_swap(__float_as_uint(ps), __float_as_uint(ps), false, false);
    ps = __uint_as_float(rr[0]) + __uint_as_float(rr[1]); }
  l_reg = l_reg * alpha + ps;
#define PK4(P, BASE, OUT) do { unsigned a0 = cvtpk(P[BASE + 0], P[BASE + 1]), a1 = cvtpk(P[BASE + 2], P[BASE + 3]);   \
    unsigned b0 = cvtpk(P[BASE + 4], P[BASE + 5]), b1 = cvtpk(P[BASE + 6], P[BASE + 7]);                              \
    auto r0 = __builtin_amdgcn_permlane32_swap(a0, b0, false, false); auto r1 = __builtin_amdgcn_permlane32_swap(a1, b1, false, false); \
    u32x4 w = {r0[0], r1[0], r0[1], r1[1]}; OUT = *reinterpret_cast<bf16x8*>(&w); } while (0)
  PK4(p0, 0, pa0); PK4(p0, 8, pa1); PK4(p1, 0, pa2); PK4(p1, 8, pa3);
#undef PK4
}
__device__ __forceinline__ void qkt(f32x16& p0, f32x16& p1, const bf16* Ks, const bf16x8* qr, int r32, int hi) {
  p0 = f32x16{}; p1 = f32x16{};
  for (int d0 = 0; d0 < 8; ++d0) { int cb = (d0 * 16 + hi * 8) * 2;
    bf16x8 b0 = *reinterpret_cast<const bf16x8*>((const char*)Ks + KSWZ(r32, cb));
    bf16x8 b1 = *reinterpret_cast<const bf16x8*>((const char*)Ks + KSWZ(32 + r32, cb));
    p0 = __builtin_amdgcn_mfma_f32_32x32x16_bf16(b0, qr[d0], p0, 0, 0, 0);
    p1 = __builtin_amdgcn_mfma_f32_32x32x16_bf16(b1, qr[d0], p1, 0, 0, 0); }
}
__device__ __forceinline__ int v_st(int k, int c) { const int kk = (k & ~0xC) | ((k & 4) << 1) | ((k & 8) >> 1); return ((kk >> 3) * 4 + (c >> 5)) * 512 + ((kk & 7) * 32 + (c & 31)) * 2; }
__device__ __forceinline__ int v_rd_base(int lane) { return ((lane & 3) << 3) | (((lane >> 2) & 3) << 6) | (((lane >> 4) & 1) << 5) | (((lane >> 5) & 1) << 8); }
constexpr int v_rd_off(int d0, int ks, int half) { return d0 * 512 + ks * 4096 + half * 2048; }
template <int OFF> __device__ __forceinline__ s16x4 tr_read(int vb) {
  s16x4 r; asm volatile("ds_read_b64_tr_b16 %0, %1 offset:%2" : "=&v"(r) : "v"(vb), "i"(OFF) : "memory"); return r;
}
template <int D0> __device__ __forceinline__ void pv_one(f32x16& od, int vb, bf16x8 pa0, bf16x8 pa1, bf16x8 pa2, bf16x8 pa3) {
  const s16x4 l0 = tr_read<v_rd_off(D0, 0, 0)>(vb), h0 = tr_read<v_rd_off(D0, 0, 1)>(vb), l1 = tr_read<v_rd_off(D0, 1, 0)>(vb), h1 = tr_read<v_rd_off(D0, 1, 1)>(vb);
  const s16x4 l2 = tr_read<v_rd_off(D0, 2, 0)>(vb), h2 = tr_read<v_rd_off(D0, 2, 1)>(vb), l3 = tr_read<v_rd_off(D0, 3, 0)>(vb), h3 = tr_read<v_rd_off(D0, 3, 1)>(vb);
  asm volatile("s_waitcnt lgkmcnt(0)" ::: "memory"); SBAR();
#define PK(L, H) (bf16x8){L[0], L[1], L[2], L[3], H[0], H[1], H[2], H[3]}
  od = __builtin_amdgcn_mfma_f32_32x32x16_bf16(pa0, PK(l0, h0), od, 0, 0, 0);
  od = __builtin_amdgcn_mfma_f32_32x32x16_bf16(pa1, PK(l1, h1), od, 0, 0, 0);
  od = __builtin_amdgcn_mfma_f32_32x32x16_bf16(pa2, PK(l2, h2), od, 0, 0, 0);
  od = __builtin_amdgcn_mfma_f32_32x32x16_bf16(pa3, PK(l3, h3), od, 0, 0, 0);
#undef PK
}
__device__ __forceinline__ void pv_d0(f32x16* o, int vb, bf16x8 pa0, bf16x8 pa1, bf16x8 pa2, bf16x8 pa3) {
  pv_one<0>(o[0], vb, pa0, pa1, pa2, pa3); pv_one<1>(o[1], vb, pa0, pa1, pa2, pa3); pv_one<2>(o[2], vb, pa0, pa1, pa2, pa3); pv_one<3>(o[3], vb, pa0, pa1, pa2, pa3);
}
__global__ __launch_bounds__(64 * NW, 1)
void attn_fwd(const bf16* __restrict__ Q, const bf16* __restrict__ K, const bf16* __restrict__ V, bf16* __restrict__ O) {
  extern __shared__ __attribute__((aligned(16))) char lds[];
  const int LDQ = D, LDK = D, LDO = D;
  const long g = blockIdx.y;
  const int row0 = blockIdx.x * 256;
  const bf16* Qb = Q + g * ROWS * D; const bf16* Kh = K + g * SKV * D; const bf16* Vh = V + g * SKV * D; bf16* Ob = O + g * ROWS * D;
  const int tid = threadIdx.x, wid = tid >> 6, lane = tid & 63, r32 = lane & 31, hi = lane >> 5;
  bf16* V_lds = (bf16*)lds; bf16* K_lds = (bf16*)(lds + 2 * SHM_V);
  float* ws = (float*)(lds + 2 * SHM_V + 2 * SHM_K) + wid * 64; float* li_l = ws; float* al_l = ws + 32;
  float m_reg = -1e30f, l_reg = 0; f32x16 o[4] = {}; bf16x8 qr[8];
  int wrow = row0 + wid * QBLK; const bool live = wrow < ROWS; if (!live) wrow = 0;
  const bf16* Qw = Qb + (long)(wrow + r32) * LDQ + hi * 8;
#pragma unroll
  for (int d0 = 0; d0 < 8; ++d0) qr[d0] = ld8(Qw + d0 * 16);
  const int sr = tid >> 4, sc = (tid & 15) * 8, vst0 = v_st(sr, sc), vst1 = v_st(32 + sr, sc);
  const int vb0 = (int)(uintptr_t)V_lds + v_rd_base(lane);
  struct { bf16x8 vs0, vs1, ks0, ks1; } sr_[2];
#define SLOAD(i, k0) do { sr_[i].vs0 = ld8(&Vh[(long)((k0) + sr) * LDK + sc]); sr_[i].vs1 = ld8(&Vh[(long)((k0) + 32 + sr) * LDK + sc]); \
    sr_[i].ks0 = ld8(&Kh[(long)((k0) + sr) * LDK + sc]); sr_[i].ks1 = ld8(&Kh[(long)((k0) + 32 + sr) * LDK + sc]); } while (0)
#define SWRITE(b, i) do { *(bf16x8*)((char*)V_lds + (b) * SHM_V + vst0) = sr_[i].vs0;          \
    *(bf16x8*)((char*)V_lds + (b) * SHM_V + vst1) = sr_[i].vs1; int kc = sc * 2;               \
    *(bf16x8*)((char*)K_lds + (b) * SHM_K + KSWZ(sr, kc)) = sr_[i].ks0;                       \
    *(bf16x8*)((char*)K_lds + (b) * SHM_K + KSWZ(32 + sr, kc)) = sr_[i].ks1; } while (0)
#define SWAIT() asm volatile("s_waitcnt vmcnt(4)" ::: "memory")
#define RESC(a) do { if (__any((a) < 1.f)) { if (hi == 0) al_l[r32] = (a); asm volatile("s_waitcnt lgkmcnt(0)" ::: "memory"); \
    for (int d = 0; d < 4; ++d) for (int r = 0; r < 16; ++r) o[d][r] *= al_l[crow(r, hi)]; } } while (0)
  f32x16 pA0, pA1, pB0, pB1; float mnA, mnB, alA, alB; bf16x8 pa0, pa1, pa2, pa3; const int NT = SKV / KVBLK;
  constexpr int SE = 0, SO = 1;
  SLOAD(SE, 0); asm volatile("s_waitcnt vmcnt(0)" ::: "memory"); SWRITE(0, SE); __syncthreads();
  qkt(pA0, pA1, K_lds, qr, r32, hi); partialSM(pA0, pA1, m_reg, mnA, alA);
  SLOAD(SO, KVBLK); SLOAD(SE, 2 * KVBLK);
  SWAIT(); SWRITE(1, SO); __syncthreads();
  for (int j = 1; j + 1 < NT; j += 2) {
    SBAR(); qkt(pB0, pB1, (bf16*)((char*)K_lds + SHM_K), qr, r32, hi);
    finishSM(pA0, pA1, alA, l_reg, pa0, pa1, pa2, pa3); SBAR();
    SLOAD(SO, (j + 2) * KVBLK); SBAR();
    pv_d0(o, vb0, pa0, pa1, pa2, pa3); partialSM(pB0, pB1, m_reg, mnB, alB);
    __syncthreads(); SWAIT(); SWRITE(0, SE);
    RESC(alB); __syncthreads();
    SBAR(); qkt(pA0, pA1, K_lds, qr, r32, hi);
    finishSM(pB0, pB1, alB, l_reg, pa0, pa1, pa2, pa3); SBAR();
    if (j + 3 < NT) SLOAD(SE, (j + 3) * KVBLK); SBAR();
    pv_d0(o, vb0 + (int)SHM_V, pa0, pa1, pa2, pa3); partialSM(pA0, pA1, m_reg, mnA, alA);
    __syncthreads(); SWAIT(); SWRITE(1, SO);
    RESC(alA); __syncthreads();
  }
  SBAR(); qkt(pB0, pB1, (bf16*)((char*)K_lds + SHM_K), qr, r32, hi);
  finishSM(pA0, pA1, alA, l_reg, pa0, pa1, pa2, pa3); SBAR();
  pv_d0(o, vb0, pa0, pa1, pa2, pa3); partialSM(pB0, pB1, m_reg, mnB, alB);
  __syncthreads(); RESC(alB);
  finishSM(pB0, pB1, alB, l_reg, pa0, pa1, pa2, pa3); SBAR();
  pv_d0(o, vb0 + (int)SHM_V, pa0, pa1, pa2, pa3);
  if (hi == 0) li_l[r32] = l_reg; asm volatile("s_waitcnt lgkmcnt(0)" ::: "memory");
  float rli[16];
#pragma unroll
  for (int r = 0; r < 16; ++r) rli[r] = __builtin_amdgcn_rcpf(li_l[crow(r, hi)]);
  bf16* Ow = Ob + (long)wrow * LDO;
  if (live) {
#pragma unroll
  for (int r = 0; r < 16; ++r) { int orow = crow(r, hi);
    for (int d0 = 0; d0 < 4; ++d0) Ow[(long)orow * LDO + d0 * 32 + r32] = __float2bfloat16(o[d0][r] * rli[r]); }
  }
}
extern "C" void kernel_launch(void* const* d_in, const int* in_sizes, int n_in, void* d_out, int out_size, void* d_ws, size_t ws_size, hipStream_t stream) {
  hipFuncSetAttribute((const void*)attn_fwd, hipFuncAttributeMaxDynamicSharedMemorySize, (int)SHM_ATTN);
  hipLaunchKernelGGL(attn_fwd, dim3(3, B * HKV), dim3(64 * NW), SHM_ATTN, stream,
                     (const bf16*)d_in[0], (const bf16*)d_in[1], (const bf16*)d_in[2], (bf16*)d_out);
}
```

```cpp
#include <hip/hip_runtime.h>
#include <hip/hip_bf16.h>
#include <stdint.h>
#define STAMPS 0

constexpr int SQ = 336, SK = 1024, DH = 128;

__device__ __forceinline__ void slow_row(const unsigned short* __restrict__ q, const unsigned short* __restrict__ k,
                                         const unsigned short* __restrict__ v, unsigned short* __restrict__ o, int row, int lane) {
  const float c = 0.12751743082f;
  const unsigned qq = *(const unsigned*)(q + row * DH + 2 * lane);
  const float q0 = __uint_as_float(qq << 16), q1 = __uint_as_float(qq & 0xffff0000u);
  float m = -3.0e38f, l = 0.f, o0 = 0.f, o1 = 0.f;
  for (int j = 0; j < SK; ++j) {
    const unsigned kk = *(const unsigned*)(k + j * DH + 2 * lane);
    float s = q0 * __uint_as_float(kk << 16) + q1 * __uint_as_float(kk & 0xffff0000u);
#pragma unroll
    for (int d = 1; d < 64; d <<= 1) s += __shfl_xor(s, d, 64);
    s *= c;
    const float mn = fmaxf(m, s);
    const float a = __builtin_amdgcn_exp2f(m - mn), p = __builtin_amdgcn_exp2f(s - mn);
    const unsigned vv = *(const unsigned*)(v + j * DH + 2 * lane);
    l = l * a + p;
    o0 = o0 * a + p * __uint_as_float(vv << 16);
    o1 = o1 * a + p * __uint_as_float(vv & 0xffff0000u);
    m = mn;
  }
  const float r = 1.f / l;
  __hip_bfloat16 b0 = __float2bfloat16(o0 * r), b1 = __float2bfloat16(o1 * r);
  unsigned w = (unsigned)(*(unsigned short*)&b0) | ((unsigned)(*(unsigned short*)&b1) << 16);
  *(unsigned*)(o + row * DH + 2 * lane) = w;
}

__global__ __attribute__((aligned(4096))) __launch_bounds__(256, 1) void gqa_attn_kernel(const unsigned short* __restrict__ Q, const unsigned short* __restrict__ K,
                                                          const unsigned short* __restrict__ V, unsigned short* __restrict__ O
) {
  __shared__ __attribute__((aligned(1024))) char lds[163840];
  const unsigned bid = blockIdx.x;
  const unsigned grp = (bid & 7) * 16 + (bid >> 4), head = grp * 2 + ((bid >> 3) & 1);
  const unsigned w = __builtin_amdgcn_readfirstlane(threadIdx.x >> 6);
  unsigned flag;
  asm volatile(
      "s_load_dwordx2 s[44:45], %[ka], 0\n"
      "s_load_dwordx2 s[36:37], %[ka], 8\n"
      "s_load_dwordx2 s[40:41], %[ka], 16\n"
      "s_load_dwordx2 s[48:49], %[ka], 24\n"
      "s_mov_b32 s52, %[w]\n"
      "v_mbcnt_lo_u32_b32 v254, -1, 0\n"
      "v_mbcnt_hi_u32_b32 v254, -1, v254\n"
      "s_lshr_b32 s100, %[bid], 3\n"
      "s_lshl2_add_u32 s100, s100, s52\n"
      "s_lshl_b32 s100, s100, 7\n"
      "s_getpc_b64 s[96:97]\n"
      "s_and_b32 s97, s97, 0xffff\n"
      "s_mov_b32 s98, 24576\n"
      "s_mov_b32 s99, 0x20000\n"
      "v_lshl_add_u32 v212, v254, 14, s100\n"
      "buffer_load_dword v212, v212, s[96:99], 0 offen\n"
      "s_lshr_b32 s99, %[bid], 4\n"
      "s_and_b32 s98, %[bid], 7\n"
      "s_lshl4_add_u32 s99, s98, s99\n"
      "s_bfe_u32 s98, %[bid], 0x10003\n"
      "s_lshl1_add_u32 s98, s99, s98\n"
      "s_mul_i32 s98, s98, 86016\n"
      "s_lshl_b32 s99, s99, 18\n"
      "s_mov_b32 s46, 86016\n"
      "s_mov_b32 s47, 0x20000\n"
      "s_mov_b32 s38, 262144\n"
      "s_mov_b32 s39, 0x20000\n"
      "s_mov_b32 s42, 262144\n"
      "s_mov_b32 s43, 0x20000\n"
      "s_mov_b32 s50, 86016\n"
      "s_mov_b32 s51, 0x20000\n"
      "v_bfe_u32 v0, v254, 2, 2\n"
      "v_lshrrev_b32 v1, 4, v254\n"
      "v_and_b32 v2, 3, v254\n"
      "v_lshlrev_b32 v248, 8, v0\n"
      "v_lshl_add_u32 v248, v1, 6, v248\n"
      "v_lshl_add_u32 v248, v2, 4, v248\n"
      "v_xor_b32 v249, 32, v248\n"
      "s_lshl_b32 s58, s52, 12\n"
      "s_lshl_b32 s62, s52, 13\n"
      "s_mul_i32 s63, s52, 20480\n"
      "s_and_b32 s60, s52, 2\n"
      "s_lshl_b32 s60, s60, 4\n"
      "v_xor_b32 v8, s60, v248\n"
      "s_lshl_b32 s60, s52, 10\n"
      "s_add_u32 s69, s60, 81920\n"
      "s_add_u32 s64, s63, 0\n"
      "s_add_u32 s65, s63, 4096\n"
      "s_add_u32 s66, s63, 8192\n"
      "s_add_u32 s67, s63, 12288\n"
      "s_add_u32 s68, s63, 16384\n"
      "v_lshlrev_b32 v214, 4, v1\n"
      "v_lshlrev_b32 v250, 10, v0\n"
      "v_lshl_add_u32 v250, v2, 6, v250\n"
      "v_and_b32 v3, 2, v0\n"
      "v_xor_b32 v3, v3, v1\n"
      "v_lshl_add_u32 v250, v3, 4, v250\n"
      "v_add_u32 v246, s62, v250\n"
      "v_add_u32 v252, 0x10000, v246\n"
      "v_add_u32 v253, 0x20000, v246\n"
      "v_add_u32 v213, 0x20000, v250\n"
      "v_and_b32 v3, 15, v254\n"
      "v_lshlrev_b32 v251, 4, v3\n"
      "v_lshl_add_u32 v251, v1, 10, v251\n"
      "v_add_u32 v251, 0x10000, v251\n"
      "v_add_u32 v247, s62, v251\n"
      "v_and_b32 v3, 2, v0\n"
      "v_lshlrev_b32 v3, 1, v3\n"
      "v_lshl_add_u32 v5, v1, 1, v0\n"
      "v_sub_u32 v5, v5, v3\n"
      "v_mov_b32 v6, 0xb9d83e03\n"
      "v_cmp_eq_u32 vcc, 0, v5\n"
      "s_nop 1\n"
      "v_cndmask_b32 v7, 0, v6, vcc\n"
      "v_accvgpr_write_b32 a16, v7\n"
      "v_accvgpr_write_b32 a17, 0\n"
      "v_accvgpr_write_b32 a21, v7\n"
      "v_accvgpr_write_b32 a20, 0\n"
      "v_cmp_eq_u32 vcc, 1, v5\n"
      "s_nop 1\n"
      "v_cndmask_b32 v7, 0, v6, vcc\n"
      "v_accvgpr_write_b32 a18, v7\n"
      "v_accvgpr_write_b32 a19, 0\n"
      "v_accvgpr_write_b32 a23, v7\n"
      "s_add_u32 m0, s60, 131072\n"
      "s_waitcnt lgkmcnt(0)\n"
      "s_load_dword s101, s[36:37], s99\n"
      "s_load_dword s101, s[44:45], s98\n"
      "s_load_dwordx8 s[28:35], s[40:41], s99\n"
      "s_load_dword s101, s[48:49], s98\n"
      "s_add_u32 s36, s36, s99\n"
      "s_addc_u32 s37, s37, 0\n"
      "s_and_b32 s37, s37, 0xffff\n"
      "s_mov_b32 m0, s58\n"
      "s_nop 0\n"
      "buffer_load_dwordx4 v248, s[36:39], s58 offen offset:0 nt lds\n"
      "buffer_load_dwordx4 v248, s[36:39], s58 offen offset:1024 nt lds\n"
      "buffer_load_dwordx4 v249, s[36:39], s58 offen offset:2048 nt lds\n"
      "buffer_load_dwordx4 v249, s[36:39], s58 offen offset:3072 nt lds\n"
      "s_add_u32 s44, s44, s98\n"
      "s_addc_u32 s45, s45, 0\n"
      "s_and_b32 s45, s45, 0xffff\n"
      "s_add_u32 m0, s60, 131072\n"
      "s_nop 0\n"
      "buffer_load_dwordx4 v8, s[44:47], s69 offen offset:0 nt lds\n"
      "s_add_u32 m0, s62, 32768\n"
      "s_nop 0\n"
      "buffer_load_dwordx4 v248, s[44:47], s64 offen offset:0 nt lds\n"
      "buffer_load_dwordx4 v248, s[44:47], s64 offen offset:1024 nt lds\n"
      "buffer_load_dwordx4 v249, s[44:47], s64 offen offset:2048 nt lds\n"
      "buffer_load_dwordx4 v249, s[44:47], s64 offen offset:3072 nt lds\n"
      "s_add_u32 s40, s40, s99\n"
      "s_addc_u32 s41, s41, 0\n"
      "s_and_b32 s41, s41, 0xffff\n"
      "s_add_u32 s48, s48, s98\n"
      "s_addc_u32 s49, s49, 0\n"
      "s_and_b32 s49, s49, 0xffff\n"
      "v_accvgpr_write_b32 a22, 0\n"
      "v_cmp_eq_u32 vcc, 4, v5\n"
      "s_nop 1\n"
      "v_cndmask_b32 v7, 0, v6, vcc\n"
      "v_accvgpr_write_b32 a24, v7\n"
      "v_accvgpr_write_b32 a25, 0\n"
      "v_accvgpr_write_b32 a29, v7\n"
      "v_accvgpr_write_b32 a28, 0\n"
      "v_cmp_eq_u32 vcc, 5, v5\n"
      "s_nop 1\n"
      "v_cndmask_b32 v7, 0, v6, vcc\n"
      "v_accvgpr_write_b32 a26, v7\n"
      "v_accvgpr_write_b32 a27, 0\n"
      "v_accvgpr_write_b32 a31, v7\n"
      "v_accvgpr_write_b32 a30, 0\n"
      "v_mul_u32_u24 v245, 0x5555, v2\n"
      "v_mov_b32 v192, 0xc28a0000\n"
      "v_mov_b32 v193, 0xc28a0000\n"
      "v_mov_b32 v194, 0xc28a0000\n"
      "v_mov_b32 v195, 0xc28a0000\n"
      "v_mov_b32 v240, 0x3f003f00\n"
      "v_mov_b32 v241, 0x3f003f00\n"
      "v_mov_b32 v242, 0x3f003f00\n"
      "v_mov_b32 v243, 0x3f003f00\n"
      "v_and_b32 v3, 1, v0\n"
      "v_mul_u32_u24 v3, 0xaaaa, v3\n"
      "v_or_b32 v244, 0x4444, v3\n"
      "v_mov_b32 v216, 0\n"
      "v_mov_b32 v217, 0\n"
      "v_mov_b32 v218, 0\n"
      "v_mov_b32 v219, 0\n"
      "v_mov_b32 v220, 0\n"
      "v_mov_b32 v221, 0\n"
      "v_mov_b32 v222, 0\n"
      "v_mov_b32 v223, 0\n"
      "v_mov_b32 v224, 0\n"
      "v_mov_b32 v225, 0\n"
      "v_mov_b32 v226, 0\n"
      "v_mov_b32 v227, 0\n"
      "v_mov_b32 v228, 0\n"
      "v_mov_b32 v229, 0\n"
      "v_mov_b32 v230, 0\n"
      "v_mov_b32 v231, 0\n"
      "v_mov_b32 v232, 0\n"
      "v_mov_b32 v233, 0\n"
      "v_mov_b32 v234, 0\n"
      "v_mov_b32 v235, 0\n"
      "v_mov_b32 v236, 0\n"
      "v_mov_b32 v237, 0\n"
      "v_mov_b32 v238, 0\n"
      "v_mov_b32 v239, 0\n"
      "s_mov_b64 s[56:57], 0\n"
      "s_mov_b64 vcc, 0\n"
      "s_add_u32 s59, s58, 16384\n"
      "s_add_u32 s53, s62, 32768\n"
      "s_add_u32 s54, s62, 36864\n"
      "s_add_u32 s55, s62, 32768\n"
      "s_add_u32 s61, s62, 266240\n"
      "v_lshlrev_b32 v254, 2, v254\n"
      "v_add_u32 v254, 139264, v254\n"
      "v_mov_b32 v255, 1\n"
      "ds_write_b32 v254, v216 offset:0\n"
      "s_mov_b32 s100, 4\n"
      "v_mov_b32 v3, -1\n"
      "ds_write_b32 v254, v3 offset:256\n"
      "s_nop 0\n"
      "v_mov_b64 v[190:191], 0\n"
      "v_mov_b64 v[188:189], 0\n"
      "v_mov_b64 v[186:187], 0\n"
      "v_mov_b64 v[184:185], 0\n"
      "v_mov_b64 v[182:183], 0\n"
      "v_mov_b64 v[180:181], 0\n"
      "v_mov_b64 v[178:179], 0\n"
      "v_mov_b64 v[176:177], 0\n"
      "v_mov_b64 v[174:175], 0\n"
      "v_mov_b64 v[172:173], 0\n"
      "v_mov_b64 v[170:171], 0\n"
      "v_mov_b64 v[168:169], 0\n"
      "v_mov_b64 v[166:167], 0\n"
      "v_mov_b64 v[164:165], 0\n"
      "v_mov_b64 v[162:163], 0\n"
      "v_mov_b64 v[160:161], 0\n"
      "v_mov_b64 v[158:159], 0\n"
      "v_mov_b64 v[156:157], 0\n"
      "v_mov_b64 v[154:155], 0\n"
      "v_mov_b64 v[152:153], 0\n"
      "v_mov_b64 v[150:151], 0\n"
      "v_mov_b64 v[148:149], 0\n"
      "v_mov_b64 v[146:147], 0\n"
      "v_mov_b64 v[144:145], 0\n"
      "v_mov_b64 v[142:143], 0\n"
      "v_mov_b64 v[140:141], 0\n"
      "v_mov_b64 v[138:139], 0\n"
      "v_mov_b64 v[136:137], 0\n"
      "v_mov_b64 v[134:135], 0\n"
      "v_mov_b64 v[132:133], 0\n"
      "v_mov_b64 v[130:131], 0\n"
      "v_mov_b64 v[128:129], 0\n"
      "v_mov_b64 v[126:127], 0\n"
      "v_mov_b64 v[124:125], 0\n"
      "v_mov_b64 v[122:123], 0\n"
      "v_mov_b64 v[120:121], 0\n"
      "v_mov_b64 v[118:119], 0\n"
      "v_mov_b64 v[116:117], 0\n"
      "v_mov_b64 v[114:115], 0\n"
      "v_mov_b64 v[112:113], 0\n"
      "v_mov_b64 v[110:111], 0\n"
      "v_mov_b64 v[108:109], 0\n"
      "v_mov_b64 v[106:107], 0\n"
      "v_mov_b64 v[104:105], 0\n"
      "v_mov_b64 v[102:103], 0\n"
      "v_mov_b64 v[100:101], 0\n"
      "v_mov_b64 v[98:99], 0\n"
      "v_mov_b64 v[96:97], 0\n"
      "v_mov_b64 v[94:95], 0\n"
      "v_mov_b64 v[92:93], 0\n"
      "v_mov_b64 v[90:91], 0\n"
      "v_mov_b64 v[88:89], 0\n"
      "v_mov_b64 v[86:87], 0\n"
      "v_mov_b64 v[84:85], 0\n"
      "v_mov_b64 v[82:83], 0\n"
      "v_mov_b64 v[80:81], 0\n"
      "v_mov_b64 v[78:79], 0\n"
      "v_mov_b64 v[76:77], 0\n"
      "v_mov_b64 v[74:75], 0\n"
      "v_mov_b64 v[72:73], 0\n"
      "v_mov_b64 v[70:71], 0\n"
      "v_mov_b64 v[68:69], 0\n"
      "v_mov_b64 v[66:67], 0\n"
      "v_mov_b64 v[64:65], 0\n"
      "v_mov_b64 v[62:63], 0\n"
      "v_mov_b64 v[60:61], 0\n"
      "v_mov_b64 v[58:59], 0\n"
      "v_mov_b64 v[56:57], 0\n"
      "v_mov_b64 v[54:55], 0\n"
      "v_mov_b64 v[52:53], 0\n"
      "v_mov_b64 v[50:51], 0\n"
      "v_mov_b64 v[48:49], 0\n"
      "v_mov_b64 v[46:47], 0\n"
      "v_mov_b64 v[44:45], 0\n"
      "v_mov_b64 v[42:43], 0\n"
      "v_mov_b64 v[40:41], 0\n"
      "v_mov_b64 v[38:39], 0\n"
      "v_mov_b64 v[36:37], 0\n"
      "v_mov_b64 v[34:35], 0\n"
      "v_mov_b64 v[32:33], 0\n"
      "v_mov_b64 v[30:31], 0\n"
      "v_mov_b64 v[28:29], 0\n"
      "v_mov_b64 v[26:27], 0\n"
      "v_mov_b64 v[24:25], 0\n"
      "v_mov_b64 v[22:23], 0\n"
      "v_mov_b64 v[20:21], 0\n"
      "v_mov_b64 v[18:19], 0\n"
      "v_mov_b64 v[16:17], 0\n"
      "v_mov_b64 v[14:15], 0\n"
      "v_mov_b64 v[12:13], 0\n"
      "v_mov_b64 v[10:11], 0\n"
      "v_mov_b64 v[8:9], 0\n"
      "v_mov_b64 v[6:7], 0\n"
      "v_mov_b64 v[4:5], 0\n"
      "v_mov_b64 v[2:3], 0\n"
      "v_mov_b64 v[0:1], 0\n"
      "s_add_u32 m0, s58, 65536\n"
      "s_nop 0\n"
      "buffer_load_dwordx4 v248, s[40:43], s58 offen offset:0 nt lds\n"
      "buffer_load_dwordx4 v248, s[40:43], s58 offen offset:1024 nt lds\n"
      "buffer_load_dwordx4 v248, s[40:43], s58 offen offset:2048 nt lds\n"
      "buffer_load_dwordx4 v248, s[40:43], s58 offen offset:3072 nt lds\n"
      "TW0%=:\n"
      "s_cmp_lg_u32 s52, 0\n"
      "s_cbranch_scc1 TW1%=\n"
      "s_call_b64 s[94:95], PAD0%=\n"
      "s_call_b64 s[94:95], PAD4%=\n"
      "s_branch TW4%=\n"
      "TW1%=:\n"
      "s_cmp_lg_u32 s52, 1\n"
      "s_cbranch_scc1 TW2%=\n"
      "s_call_b64 s[94:95], PAD1%=\n"
      "s_branch TW4%=\n"
      "TW2%=:\n"
      "s_cmp_lg_u32 s52, 2\n"
      "s_cbranch_scc1 TW3%=\n"
      "s_call_b64 s[94:95], PAD2%=\n"
      "s_branch TW4%=\n"
      "SPINA0x%=:\n"
      "ds_read_b32 v253, v254\n"
      "s_waitcnt lgkmcnt(0)\n"
      "v_cmp_gt_u32 vcc, s100, v253\n"
      "s_cbranch_vccnz SPINA0x%=\n"
      "s_branch BACKA0x%=\n"
      "SPINB%=:\n"
      "ds_read_b32 v253, v254\n"
      "s_waitcnt lgkmcnt(0)\n"
      "v_cmp_gt_u32 vcc, s100, v253\n"
      "s_cbranch_vccnz SPINB%=\n"
      "s_branch BACKB%=\n"
      "HSPN%=:\n"
      "ds_read_b128 v[236:239], v214 offset:256\n"
      "s_waitcnt lgkmcnt(0)\n"
      "v_max3_u32 v253, v236, v237, v238\n"
      "v_max_u32 v253, v253, v239\n"
      "v_cmp_eq_u32 vcc, -1, v253\n"
      "s_nop 0\n"
      "s_cbranch_vccnz HSPN%=\n"
      "s_branch HBCK%=\n"
      "s_nop 0\n"
      "s_nop 0\n"
      "s_nop 0\n"
      "s_nop 0\n"
      "TW3%=:\n"
      "s_call_b64 s[94:95], PAD3%=\n"
      "TW4%=:\n"
      "s_add_u32 m0, s62, 36864\n"
      "s_waitcnt vmcnt(8) lgkmcnt(0)\n"
      "s_barrier\n"
      "ds_read_b128 a[112:115], v213 offset:0\n"
      "ds_read_b128 a[116:119], v213 offset:256\n"
      "ds_read_b128 a[120:123], v213 offset:512\n"
      "ds_read_b128 a[124:127], v213 offset:768\n"
      "s_waitcnt lgkmcnt(2)\n"
      "v_smfmac_f32_16x16x64_bf16 v[160:163], a[16:19], a[112:119], v245\n"
      "ds_read_b128 a[160:163], v250 offset:0\n"
      "buffer_load_dwordx4 v248, s[44:47], s65 offen offset:0 nt lds\n"
      "buffer_load_dwordx4 v248, s[44:47], s65 offen offset:1024 nt lds\n"
      "buffer_load_dwordx4 v249, s[44:47], s65 offen offset:2048 nt lds\n"
      "buffer_load_dwordx4 v249, s[44:47], s65 offen offset:3072 nt lds\n"
      "v_smfmac_f32_16x16x64_bf16 v[164:167], a[20:23], a[112:119], v245\n"
      "s_add_u32 m0, s62, 98304\n"
      "ds_read_b128 a[128:131], v250 offset:8192\n"
      "s_waitcnt vmcnt(8)\n"
      "ds_read_b128 a[32:35], v246 offset:32768\n"
      "ds_read_b128 a[36:39], v246 offset:33024\n"
      "ds_read_b128 a[40:43], v246 offset:33280\n"
      "ds_read_b128 a[44:47], v246 offset:33536\n"
      "v_smfmac_f32_16x16x64_bf16 v[168:171], a[24:27], a[112:119], v245\n"
      "ds_read_b128 a[164:167], v250 offset:256\n"
      "v_smfmac_f32_16x16x64_bf16 v[172:175], a[28:31], a[112:119], v245\n"
      "s_waitcnt lgkmcnt(7)\n"
      "ds_read_b128 a[168:171], v250 offset:512\n"
      "v_smfmac_f32_16x16x64_bf16 v[176:179], a[16:19], a[120:127], v245\n"
      "ds_read_b128 a[132:135], v250 offset:8448\n"
      "ds_read_b128 v[196:199], v214 offset:0\n"
      "v_smfmac_f32_16x16x64_bf16 v[180:183], a[20:23], a[120:127], v245\n"
      "ds_read_b128 v[200:203], v214 offset:256\n"
      "v_smfmac_f32_16x16x64_bf16 v[184:187], a[24:27], a[120:127], v245\n"
      "ds_read_b128 a[172:175], v250 offset:768\n"
      "ds_read_b128 v[204:207], v214 offset:512\n"
      "v_smfmac_f32_16x16x64_bf16 v[188:191], a[28:31], a[120:127], v245\n"
      "s_waitcnt lgkmcnt(9)\n"
      "ds_read_b128 a[176:179], v250 offset:4096\n"
      "ds_read_b128 v[208:211], v214 offset:768\n"
      "v_smfmac_f32_16x16x64_bf16 v[0:3], a[16:19], a[32:39], v245\n"
      "ds_read_b128 a[136:139], v250 offset:8704\n"
      "v_smfmac_f32_16x16x64_bf16 v[4:7], a[20:23], a[32:39], v245\n"
      "ds_read_b128 a[180:183], v250 offset:4352\n"
      "v_smfmac_f32_16x16x64_bf16 v[8:11], a[24:27], a[32:39], v245\n"
      "ds_read_b128 a[140:143], v250 offset:8960\n"
      "v_smfmac_f32_16x16x64_bf16 v[12:15], a[28:31], a[32:39], v245\n"
      "s_waitcnt lgkmcnt(10)\n"
      "ds_read_b128 a[184:187], v250 offset:4608\n"
      "v_smfmac_f32_16x16x64_bf16 v[16:19], a[16:19], a[40:47], v245\n"
      "ds_read_b128 a[188:191], v250 offset:4864\n"
      "v_smfmac_f32_16x16x64_bf16 v[20:23], a[20:23], a[40:47], v245\n"
      "ds_read_b128 a[144:147], v250 offset:12288\n"
      "v_smfmac_f32_16x16x64_bf16 v[24:27], a[24:27], a[40:47], v245\n"
      "s_waitcnt lgkmcnt(8)\n"
      "ds_read_b128 a[148:151], v250 offset:12544\n"
      "v_xor_b32 v196, 0x80008000, v196\n"
      "v_accvgpr_write_b32 a0, v196\n"
      "v_xor_b32 v197, 0x80008000, v197\n"
      "v_accvgpr_write_b32 a1, v197\n"
      "v_xor_b32 v198, 0x80008000, v198\n"
      "v_accvgpr_write_b32 a2, v198\n"
      "v_xor_b32 v199, 0x80008000, v199\n"
      "v_accvgpr_write_b32 a3, v199\n"
      "v_cvt_pk_bf16_f32 v160, v160, v161\n"
      "v_cvt_pk_bf16_f32 v162, v162, v163\n"
      "v_cvt_pk_bf16_f32 v164, v164, v165\n"
      "v_cvt_pk_bf16_f32 v166, v166, v167\n"
      "v_accvgpr_write_b32 a112, v160\n"
      "v_accvgpr_write_b32 a113, v162\n"
      "v_accvgpr_write_b32 a114, v164\n"
      "v_accvgpr_write_b32 a115, v166\n"
      "v_smfmac_f32_16x16x64_bf16 v[28:31], a[28:31], a[40:47], v245\n"
      "s_waitcnt lgkmcnt(7)\n"
      "v_xor_b32 v200, 0x80008000, v200\n"
      "v_accvgpr_write_b32 a4, v200\n"
      "v_xor_b32 v201, 0x80008000, v201\n"
      "v_accvgpr_write_b32 a5, v201\n"
      "v_xor_b32 v202, 0x80008000, v202\n"
      "v_accvgpr_write_b32 a6, v202\n"
      "v_xor_b32 v203, 0x80008000, v203\n"
      "v_accvgpr_write_b32 a7, v203\n"
      "v_cvt_pk_bf16_f32 v168, v168, v169\n"
      "v_cvt_pk_bf16_f32 v170, v170, v171\n"
      "v_cvt_pk_bf16_f32 v172, v172, v173\n"
      "v_cvt_pk_bf16_f32 v174, v174, v175\n"
      "v_accvgpr_write_b32 a116, v168\n"
      "v_accvgpr_write_b32 a117, v170\n"
      "v_accvgpr_write_b32 a118, v172\n"
      "v_accvgpr_write_b32 a119, v174\n"
      "v_mfma_f32_16x16x32_bf16 v[212:215], a[0:3], a[112:115], v[192:195]\n"
      "buffer_load_dwordx4 v248, s[44:47], s66 offen offset:0 nt lds\n"
      "v_xor_b32 v204, 0x80008000, v204\n"
      "v_accvgpr_write_b32 a8, v204\n"
      "v_xor_b32 v205, 0x80008000, v205\n"
      "v_accvgpr_write_b32 a9, v205\n"
      "v_xor_b32 v206, 0x80008000, v206\n"
      "v_accvgpr_write_b32 a10, v206\n"
      "v_xor_b32 v207, 0x80008000, v207\n"
      "v_accvgpr_write_b32 a11, v207\n"
      "v_cvt_pk_bf16_f32 v176, v176, v177\n"
      "v_cvt_pk_bf16_f32 v178, v178, v179\n"
      "v_cvt_pk_bf16_f32 v180, v180, v181\n"
      "v_cvt_pk_bf16_f32 v182, v182, v183\n"
      "v_accvgpr_write_b32 a120, v176\n"
      "v_accvgpr_write_b32 a121, v178\n"
      "v_accvgpr_write_b32 a122, v180\n"
      "v_accvgpr_write_b32 a123, v182\n"
      "v_mfma_f32_16x16x32_bf16 v[212:215], a[4:7], a[116:119], v[212:215]\n"
      "buffer_load_dwordx4 v248, s[44:47], s66 offen offset:1024 nt lds\n"
      "v_xor_b32 v208, 0x80008000, v208\n"
      "v_accvgpr_write_b32 a12, v208\n"
      "v_xor_b32 v209, 0x80008000, v209\n"
      "v_accvgpr_write_b32 a13, v209\n"
      "v_xor_b32 v210, 0x80008000, v210\n"
      "v_accvgpr_write_b32 a14, v210\n"
      "v_xor_b32 v211, 0x80008000, v211\n"
      "v_accvgpr_write_b32 a15, v211\n"
      "v_cvt_pk_bf16_f32 v184, v184, v185\n"
      "v_cvt_pk_bf16_f32 v186, v186, v187\n"
      "v_cvt_pk_bf16_f32 v188, v188, v189\n"
      "v_cvt_pk_bf16_f32 v190, v190, v191\n"
      "v_accvgpr_write_b32 a124, v184\n"
      "v_accvgpr_write_b32 a125, v186\n"
      "v_accvgpr_write_b32 a126, v188\n"
      "v_accvgpr_write_b32 a127, v190\n"
      "v_mfma_f32_16x16x32_bf16 v[212:215], a[8:11], a[120:123], v[212:215]\n"
      "buffer_load_dwordx4 v249, s[44:47], s66 offen offset:2048 nt lds\n"
      "v_cvt_pk_bf16_f32 v0, v0, v1\n"
      "v_cvt_pk_bf16_f32 v2, v2, v3\n"
      "v_cvt_pk_bf16_f32 v4, v4, v5\n"
      "v_cvt_pk_bf16_f32 v6, v6, v7\n"
      "v_accvgpr_write_b32 a32, v0\n"
      "v_accvgpr_write_b32 a33, v2\n"
      "v_accvgpr_write_b32 a34, v4\n"
      "v_accvgpr_write_b32 a35, v6\n"
      "v_mfma_f32_16x16x32_bf16 v[212:215], a[12:15], a[124:127], v[212:215]\n"
      "buffer_load_dwordx4 v249, s[44:47], s66 offen offset:3072 nt lds\n"
      "v_cvt_pk_bf16_f32 v8, v8, v9\n"
      "v_cvt_pk_bf16_f32 v10, v10, v11\n"
      "v_cvt_pk_bf16_f32 v12, v12, v13\n"
      "v_cvt_pk_bf16_f32 v14, v14, v15\n"
      "v_accvgpr_write_b32 a36, v8\n"
      "v_accvgpr_write_b32 a37, v10\n"
      "v_accvgpr_write_b32 a38, v12\n"
      "v_accvgpr_write_b32 a39, v14\n"
      "v_mfma_f32_16x16x32_bf16 v[196:199], a[0:3], a[32:35], v[192:195]\n"
      "s_and_b32 s28, s28, 0x7f800000\n"
      "ds_read_b128 a[152:155], v250 offset:12800\n"
      "v_cvt_pk_bf16_f32 v16, v16, v17\n"
      "v_cvt_pk_bf16_f32 v18, v18, v19\n"
      "v_cvt_pk_bf16_f32 v20, v20, v21\n"
      "v_cvt_pk_bf16_f32 v22, v22, v23\n"
      "v_accvgpr_write_b32 a40, v16\n"
      "v_accvgpr_write_b32 a41, v18\n"
      "v_accvgpr_write_b32 a42, v20\n"
      "v_accvgpr_write_b32 a43, v22\n"
      "v_mfma_f32_16x16x32_bf16 v[196:199], a[4:7], a[36:39], v[196:199]\n"
      "s_and_b32 s29, s29, 0x7f800000\n"
      "ds_read_b128 a[156:159], v250 offset:13056\n"
      "v_cvt_pk_bf16_f32 v24, v24, v25\n"
      "v_cvt_pk_bf16_f32 v26, v26, v27\n"
      "v_cvt_pk_bf16_f32 v28, v28, v29\n"
      "v_cvt_pk_bf16_f32 v30, v30, v31\n"
      "v_accvgpr_write_b32 a44, v24\n"
      "v_accvgpr_write_b32 a45, v26\n"
      "v_accvgpr_write_b32 a46, v28\n"
      "v_accvgpr_write_b32 a47, v30\n"
      "v_mfma_f32_16x16x32_bf16 v[196:199], a[8:11], a[40:43], v[196:199]\n"
      "s_and_b32 s30, s30, 0x7f800000\n"
      "v_mfma_f32_16x16x32_bf16 v[196:199], a[12:15], a[44:47], v[196:199]\n"
      "s_and_b32 s31, s31, 0x7f800000\n"
      "v_mfma_f32_16x16x32_bf16 v[164:167], a[160:163], a[32:35], v[196:199]\n"
      "s_and_b32 s32, s32, 0x7f800000\n"
      "v_mfma_f32_16x16x32_bf16 v[164:167], a[164:167], a[36:39], v[164:167]\n"
      "s_and_b32 s33, s33, 0x7f800000\n"
      "v_mfma_f32_16x16x32_bf16 v[164:167], a[168:171], a[40:43], v[164:167]\n"
      "s_and_b32 s34, s34, 0x7f800000\n"
      "v_mfma_f32_16x16x32_bf16 v[164:167], a[172:175], a[44:47], v[164:167]\n"
      "s_and_b32 s35, s35, 0x7f800000\n"
      "s_waitcnt lgkmcnt(2)\n"
      "v_mfma_f32_16x16x32_bf16 v[160:163], a[188:191], a[44:47], v[196:199]\n"
      "s_max_u32 s28, s28, s29\n"
      "v_mfma_f32_16x16x32_bf16 v[160:163], a[184:187], a[40:43], v[160:163]\n"
      "s_max_u32 s30, s30, s31\n"
      "v_mfma_f32_16x16x32_bf16 v[160:163], a[180:183], a[36:39], v[160:163]\n"
      "s_max_u32 s32, s32, s33\n"
      "v_mfma_f32_16x16x32_bf16 v[160:163], a[176:179], a[32:35], v[160:163]\n"
      "s_max_u32 s34, s34, s35\n"
      "v_exp_f32 v164, v164\n"
      "v_mfma_f32_16x16x32_bf16 v[168:171], a[128:131], a[32:35], v[196:199]\n"
      "s_max_u32 s28, s28, s30\n"
      "v_exp_f32 v165, v165\n"
      "v_mfma_f32_16x16x32_bf16 v[168:171], a[132:135], a[36:39], v[168:171]\n"
      "s_max_u32 s32, s32, s34\n"
      "v_exp_f32 v166, v166\n"
      "v_mfma_f32_16x16x32_bf16 v[168:171], a[136:139], a[40:43], v[168:171]\n"
      "s_max_u32 s28, s28, s32\n"
      "v_exp_f32 v167, v167\n"
      "v_mfma_f32_16x16x32_bf16 v[168:171], a[140:143], a[44:47], v[168:171]\n"
      "s_sub_i32 s28, s28, 0x1d800000\n"
      "v_exp_f32 v160, v160\n"
      "s_waitcnt lgkmcnt(0)\n"
      "v_mfma_f32_16x16x32_bf16 v[172:175], a[156:159], a[44:47], v[196:199]\n"
      "s_max_i32 s28, s28, 0\n"
      "v_exp_f32 v161, v161\n"
      "v_mfma_f32_16x16x32_bf16 v[172:175], a[152:155], a[40:43], v[172:175]\n"
      "s_or_b32 s28, s28, 0x400000\n"
      "v_exp_f32 v162, v162\n"
      "v_exp_f32 v163, v163\n"
      "v_mfma_f32_16x16x32_bf16 v[172:175], a[148:151], a[36:39], v[172:175]\n"
      "s_or_b32 s29, s28, 0x80000000\n"
      "v_mov_b32 v236, s28\n"
      "v_mov_b32 v237, s28\n"
      "v_mov_b32 v238, s28\n"
      "v_mov_b32 v239, s28\n"
      "v_cvt_pk_bf16_f32 v164, v164, v165\n"
      "v_cvt_pk_bf16_f32 v165, v166, v167\n"
      "v_cvt_pk_bf16_f32 v166, v160, v161\n"
      "v_cvt_pk_bf16_f32 v167, v162, v163\n"
      "s_add_u32 m0, s62, 102400\n"
      "v_mfma_f32_16x16x32_bf16 v[172:175], a[144:147], a[32:35], v[172:175]\n"
      "v_exp_f32 v168, v168\n"
      "v_exp_f32 v169, v169\n"
      "v_exp_f32 v170, v170\n"
      "v_exp_f32 v171, v171\n"
      "v_cvt_pk_bf16_f32 v168, v168, v169\n"
      "v_cvt_pk_bf16_f32 v169, v170, v171\n"
      "s_nop 1\n"
      "v_exp_f32 v172, v172\n"
      "v_exp_f32 v173, v173\n"
      "v_exp_f32 v174, v174\n"
      "v_exp_f32 v175, v175\n"
      "v_cvt_pk_bf16_f32 v170, v172, v173\n"
      "v_cvt_pk_bf16_f32 v171, v174, v175\n"
      "s_waitcnt vmcnt(8)\n"
      "s_barrier\n"
      "buffer_load_dwordx4 v248, s[44:47], s67 offen offset:0 nt lds\n"
      "buffer_load_dwordx4 v248, s[44:47], s67 offen offset:1024 nt lds\n"
      "buffer_load_dwordx4 v249, s[44:47], s67 offen offset:2048 nt lds\n"
      "buffer_load_dwordx4 v249, s[44:47], s67 offen offset:3072 nt lds\n"
      "ds_read_b64_tr_b16 a[224:225], v251 offset:0\n"
      "ds_read_b64_tr_b16 a[226:227], v251 offset:4096\n"
      "ds_read_b64_tr_b16 a[228:229], v251 offset:8\n"
      "ds_read_b64_tr_b16 a[230:231], v251 offset:4104\n"
      "ds_read_b64_tr_b16 a[232:233], v251 offset:256\n"
      "ds_read_b64_tr_b16 a[234:235], v251 offset:4352\n"
      "ds_read_b64_tr_b16 a[236:237], v251 offset:264\n"
      "ds_read_b64_tr_b16 a[238:239], v251 offset:4360\n"
      "ds_read_b64_tr_b16 a[240:241], v251 offset:512\n"
      "ds_read_b64_tr_b16 a[242:243], v251 offset:4608\n"
      "ds_read_b64_tr_b16 a[244:245], v251 offset:520\n"
      "ds_read_b64_tr_b16 a[246:247], v251 offset:4616\n"
      "ds_read_b64_tr_b16 a[248:249], v251 offset:768\n"
      "ds_read_b64_tr_b16 a[250:251], v251 offset:4864\n"
      "ds_read_b64_tr_b16 a[252:253], v251 offset:776\n"
      "ds_read_b64_tr_b16 a[254:255], v251 offset:4872\n"
      "s_waitcnt vmcnt(8)\n"
      "ds_read_b128 a[48:51], v246 offset:36864\n"
      "ds_read_b128 a[52:55], v246 offset:37120\n"
      "s_waitcnt lgkmcnt(15)\n"
      "v_mfma_f32_16x16x32_bf16 v[0:3], v[164:167], a[224:227], v[236:239]\n"
      "s_add_u32 m0, s62, 135168\n"
      "ds_read_b64_tr_b16 a[192:193], v251 offset:8192\n"
      "ds_read_b64_tr_b16 a[194:195], v251 offset:12288\n"
      "s_waitcnt lgkmcnt(15)\n"
      "v_mfma_f32_16x16x32_bf16 v[4:7], v[164:167], a[228:231], v[236:239]\n"
      "s_waitcnt lgkmcnt(14)\n"
      "ds_read_b64_tr_b16 a[196:197], v251 offset:8200\n"
      "ds_read_b64_tr_b16 a[198:199], v251 offset:12296\n"
      "ds_read_b128 a[56:59], v246 offset:37376\n"
      "ds_read_b128 a[60:63], v246 offset:37632\n"
      "v_mfma_f32_16x16x32_bf16 v[8:11], v[164:167], a[232:235], v[236:239]\n"
      "s_waitcnt lgkmcnt(15)\n"
      "ds_read_b64_tr_b16 a[200:201], v251 offset:8448\n"
      "ds_read_b64_tr_b16 a[202:203], v251 offset:12544\n"
      "v_mfma_f32_16x16x32_bf16 v[12:15], v[164:167], a[236:239], v[236:239]\n"
      "s_waitcnt lgkmcnt(15)\n"
      "ds_read_b64_tr_b16 a[204:205], v251 offset:8456\n"
      "ds_read_b64_tr_b16 a[206:207], v251 offset:12552\n"
      "v_mfma_f32_16x16x32_bf16 v[16:19], v[164:167], a[240:243], v[236:239]\n"
      "s_waitcnt lgkmcnt(15)\n"
      "ds_read_b64_tr_b16 a[208:209], v251 offset:8704\n"
      "ds_read_b64_tr_b16 a[210:211], v251 offset:12800\n"
      "v_mfma_f32_16x16x32_bf16 v[20:23], v[164:167], a[244:247], v[236:239]\n"
      "s_waitcnt lgkmcnt(15)\n"
      "ds_read_b64_tr_b16 a[212:213], v251 offset:8712\n"
      "ds_read_b64_tr_b16 a[214:215], v251 offset:12808\n"
      "v_mfma_f32_16x16x32_bf16 v[24:27], v[164:167], a[248:251], v[236:239]\n"
      "s_waitcnt lgkmcnt(15)\n"
      "ds_read_b64_tr_b16 a[216:217], v251 offset:8960\n"
      "ds_read_b64_tr_b16 a[218:219], v251 offset:13056\n"
      "v_mfma_f32_16x16x32_bf16 v[28:31], v[164:167], a[252:255], v[236:239]\n"
      "s_waitcnt lgkmcnt(15)\n"
      "ds_read_b64_tr_b16 a[220:221], v251 offset:8968\n"
      "ds_read_b64_tr_b16 a[222:223], v251 offset:13064\n"
      "v_smfmac_f32_16x16x64_bf16 v[32:35], a[16:19], a[48:55], v245\n"
      "v_smfmac_f32_16x16x64_bf16 v[36:39], a[20:23], a[48:55], v245\n"
      "v_smfmac_f32_16x16x64_bf16 v[40:43], a[24:27], a[48:55], v245\n"
      "v_smfmac_f32_16x16x64_bf16 v[44:47], a[28:31], a[48:55], v245\n"
      "s_waitcnt lgkmcnt(8)\n"
      "v_smfmac_f32_16x16x64_bf16 v[48:51], a[16:19], a[56:63], v245\n"
      "v_cvt_pk_bf16_f32 v32, v32, v33\n"
      "v_smfmac_f32_16x16x64_bf16 v[52:55], a[20:23], a[56:63], v245\n"
      "v_cvt_pk_bf16_f32 v34, v34, v35\n"
      "v_smfmac_f32_16x16x64_bf16 v[56:59], a[24:27], a[56:63], v245\n"
      "v_cvt_pk_bf16_f32 v36, v36, v37\n"
      "v_smfmac_f32_16x16x64_bf16 v[60:63], a[28:31], a[56:63], v245\n"
      "v_cvt_pk_bf16_f32 v38, v38, v39\n"
      "v_smfmac_f32_16x16x64_bf16 v[216:219], v[240:243], v[164:171], v244\n"
      "v_accvgpr_write_b32 a48, v32\n"
      "v_mfma_f32_16x16x32_bf16 v[0:3], v[168:171], a[192:195], v[0:3]\n"
      "v_accvgpr_write_b32 a49, v34\n"
      "v_accvgpr_write_b32 a50, v36\n"
      "v_mfma_f32_16x16x32_bf16 v[4:7], v[168:171], a[196:199], v[4:7]\n"
      "v_accvgpr_write_b32 a51, v38\n"
      "v_cvt_pk_bf16_f32 v40, v40, v41\n"
      "v_mfma_f32_16x16x32_bf16 v[8:11], v[168:171], a[200:203], v[8:11]\n"
      "v_cvt_pk_bf16_f32 v42, v42, v43\n"
      "v_cvt_pk_bf16_f32 v44, v44, v45\n"
      "v_mfma_f32_16x16x32_bf16 v[12:15], v[168:171], a[204:207], v[12:15]\n"
      "s_waitcnt lgkmcnt(0)\n"
      "v_cvt_pk_bf16_f32 v46, v46, v47\n"
      "v_accvgpr_write_b32 a52, v40\n"
      "v_mfma_f32_16x16x32_bf16 v[16:19], v[168:171], a[208:211], v[16:19]\n"
      "v_accvgpr_write_b32 a53, v42\n"
      "v_accvgpr_write_b32 a54, v44\n"
      "v_mfma_f32_16x16x32_bf16 v[20:23], v[168:171], a[212:215], v[20:23]\n"
      ".long 0xBEE500FF\n"
      "PAD0%=:\n"
      "s_setpc_b64 s[94:95]\n"
      "v_accvgpr_write_b32 a55, v46\n"
      "v_cvt_pk_bf16_f32 v48, v48, v49\n"
      "v_mfma_f32_16x16x32_bf16 v[24:27], v[168:171], a[216:219], v[24:27]\n"
      "v_cvt_pk_bf16_f32 v50, v50, v51\n"
      "v_cvt_pk_bf16_f32 v52, v52, v53\n"
      "v_mfma_f32_16x16x32_bf16 v[28:31], v[168:171], a[220:223], v[28:31]\n"
      "v_cvt_pk_bf16_f32 v54, v54, v55\n"
      "v_accvgpr_write_b32 a56, v48\n"
      "v_mfma_f32_16x16x32_bf16 v[200:203], a[0:3], a[48:51], v[192:195]\n"
      "v_accvgpr_write_b32 a57, v50\n"
      "v_accvgpr_write_b32 a58, v52\n"
      "v_accvgpr_write_b32 a59, v54\n"
      "v_mfma_f32_16x16x32_bf16 v[200:203], a[4:7], a[52:55], v[200:203]\n"
      "v_cvt_pk_bf16_f32 v56, v56, v57\n"
      "v_cvt_pk_bf16_f32 v58, v58, v59\n"
      "v_cvt_pk_bf16_f32 v60, v60, v61\n"
      "v_cvt_pk_bf16_f32 v62, v62, v63\n"
      "v_accvgpr_write_b32 a60, v56\n"
      "v_accvgpr_write_b32 a61, v58\n"
      "v_accvgpr_write_b32 a62, v60\n"
      "v_accvgpr_write_b32 a63, v62\n"
      "v_mfma_f32_16x16x32_bf16 v[200:203], a[8:11], a[56:59], v[200:203]\n"
      "v_mfma_f32_16x16x32_bf16 v[200:203], a[12:15], a[60:63], v[200:203]\n"
      "buffer_load_dwordx4 v248, s[44:47], s68 offen offset:0 nt lds\n"
      "v_mfma_f32_16x16x32_bf16 v[180:183], a[160:163], a[48:51], v[200:203]\n"
      "buffer_load_dwordx4 v248, s[44:47], s68 offen offset:1024 nt lds\n"
      "v_mfma_f32_16x16x32_bf16 v[180:183], a[164:167], a[52:55], v[180:183]\n"
      "buffer_load_dwordx4 v249, s[44:47], s68 offen offset:2048 nt lds\n"
      "v_mfma_f32_16x16x32_bf16 v[180:183], a[168:171], a[56:59], v[180:183]\n"
      "buffer_load_dwordx4 v249, s[44:47], s68 offen offset:3072 nt lds\n"
      "v_mfma_f32_16x16x32_bf16 v[180:183], a[172:175], a[60:63], v[180:183]\n"
      "s_mov_b32 m0, s59\n"
      "v_mfma_f32_16x16x32_bf16 v[176:179], a[188:191], a[60:63], v[200:203]\n"
      "v_mfma_f32_16x16x32_bf16 v[176:179], a[184:187], a[56:59], v[176:179]\n"
      "v_mfma_f32_16x16x32_bf16 v[176:179], a[180:183], a[52:55], v[176:179]\n"
      "v_mfma_f32_16x16x32_bf16 v[176:179], a[176:179], a[48:51], v[176:179]\n"
      "v_exp_f32 v180, v180\n"
      "v_mfma_f32_16x16x32_bf16 v[184:187], a[128:131], a[48:51], v[200:203]\n"
      "v_exp_f32 v181, v181\n"
      "v_mfma_f32_16x16x32_bf16 v[184:187], a[132:135], a[52:55], v[184:187]\n"
      "v_exp_f32 v182, v182\n"
      "v_mfma_f32_16x16x32_bf16 v[184:187], a[136:139], a[56:59], v[184:187]\n"
      "v_exp_f32 v183, v183\n"
      "v_mfma_f32_16x16x32_bf16 v[184:187], a[140:143], a[60:63], v[184:187]\n"
      "v_exp_f32 v176, v176\n"
      "v_mfma_f32_16x16x32_bf16 v[188:191], a[156:159], a[60:63], v[200:203]\n"
      "v_exp_f32 v177, v177\n"
      "v_mfma_f32_16x16x32_bf16 v[188:191], a[152:155], a[56:59], v[188:191]\n"
      "v_exp_f32 v178, v178\n"
      "v_exp_f32 v179, v179\n"
      "v_mfma_f32_16x16x32_bf16 v[188:191], a[148:151], a[52:55], v[188:191]\n"
      "v_cvt_pk_bf16_f32 v180, v180, v181\n"
      "v_cvt_pk_bf16_f32 v181, v182, v183\n"
      "v_cvt_pk_bf16_f32 v182, v176, v177\n"
      "v_cvt_pk_bf16_f32 v183, v178, v179\n"
      "v_mfma_f32_16x16x32_bf16 v[188:191], a[144:147], a[48:51], v[188:191]\n"
      "s_waitcnt vmcnt(8)\n"
      "ds_read_b128 a[64:67], v252 offset:32768\n"
      "ds_read_b128 a[68:71], v252 offset:33024\n"
      "v_exp_f32 v184, v184\n"
      "v_mfma_f32_16x16x32_bf16 v[32:35], v[180:183], a[224:227], v[236:239]\n"
      "v_exp_f32 v185, v185\n"
      "v_mfma_f32_16x16x32_bf16 v[36:39], v[180:183], a[228:231], v[236:239]\n"
      "ds_read_b128 a[72:75], v252 offset:33280\n"
      "ds_read_b128 a[76:79], v252 offset:33536\n"
      "v_cvt_pk_bf16_f32 v184, v184, v185\n"
      "v_mfma_f32_16x16x32_bf16 v[40:43], v[180:183], a[232:235], v[236:239]\n"
      "v_exp_f32 v186, v186\n"
      "v_mfma_f32_16x16x32_bf16 v[44:47], v[180:183], a[236:239], v[236:239]\n"
      "v_exp_f32 v187, v187\n"
      "v_mfma_f32_16x16x32_bf16 v[48:51], v[180:183], a[240:243], v[236:239]\n"
      "v_exp_f32 v188, v188\n"
      "v_mfma_f32_16x16x32_bf16 v[52:55], v[180:183], a[244:247], v[236:239]\n"
      "v_exp_f32 v189, v189\n"
      "v_mfma_f32_16x16x32_bf16 v[56:59], v[180:183], a[248:251], v[236:239]\n"
      "v_exp_f32 v190, v190\n"
      "v_mfma_f32_16x16x32_bf16 v[60:63], v[180:183], a[252:255], v[236:239]\n"
      "s_waitcnt lgkmcnt(2)\n"
      "v_exp_f32 v191, v191\n"
      "v_smfmac_f32_16x16x64_bf16 v[64:67], a[16:19], a[64:71], v245\n"
      "v_cvt_pk_bf16_f32 v185, v186, v187\n"
      "v_smfmac_f32_16x16x64_bf16 v[68:71], a[20:23], a[64:71], v245\n"
      "v_cvt_pk_bf16_f32 v186, v188, v189\n"
      "v_smfmac_f32_16x16x64_bf16 v[72:75], a[24:27], a[64:71], v245\n"
      "v_cvt_pk_bf16_f32 v187, v190, v191\n"
      "v_smfmac_f32_16x16x64_bf16 v[76:79], a[28:31], a[64:71], v245\n"
      "s_waitcnt lgkmcnt(0)\n"
      "v_smfmac_f32_16x16x64_bf16 v[80:83], a[16:19], a[72:79], v245\n"
      "v_cvt_pk_bf16_f32 v64, v64, v65\n"
      "v_smfmac_f32_16x16x64_bf16 v[84:87], a[20:23], a[72:79], v245\n"
      "v_cvt_pk_bf16_f32 v66, v66, v67\n"
      "v_smfmac_f32_16x16x64_bf16 v[88:91], a[24:27], a[72:79], v245\n"
      "v_cvt_pk_bf16_f32 v68, v68, v69\n"
      "v_smfmac_f32_16x16x64_bf16 v[92:95], a[28:31], a[72:79], v245\n"
      "v_cvt_pk_bf16_f32 v70, v70, v71\n"
      "v_smfmac_f32_16x16x64_bf16 v[220:223], v[240:243], v[180:187], v244\n"
      "v_accvgpr_write_b32 a64, v64\n"
      "v_mfma_f32_16x16x32_bf16 v[32:35], v[184:187], a[192:195], v[32:35]\n"
      "v_accvgpr_write_b32 a65, v66\n"
      "v_accvgpr_write_b32 a66, v68\n"
      "v_mfma_f32_16x16x32_bf16 v[36:39], v[184:187], a[196:199], v[36:39]\n"
      "v_accvgpr_write_b32 a67, v70\n"
      "v_cvt_pk_bf16_f32 v72, v72, v73\n"
      "v_mfma_f32_16x16x32_bf16 v[40:43], v[184:187], a[200:203], v[40:43]\n"
      "v_cvt_pk_bf16_f32 v74, v74, v75\n"
      "v_cvt_pk_bf16_f32 v76, v76, v77\n"
      "v_mfma_f32_16x16x32_bf16 v[44:47], v[184:187], a[204:207], v[44:47]\n"
      "v_cvt_pk_bf16_f32 v78, v78, v79\n"
      "v_accvgpr_write_b32 a68, v72\n"
      "v_mfma_f32_16x16x32_bf16 v[48:51], v[184:187], a[208:211], v[48:51]\n"
      "v_accvgpr_write_b32 a69, v74\n"
      "v_accvgpr_write_b32 a70, v76\n"
      "v_mfma_f32_16x16x32_bf16 v[52:55], v[184:187], a[212:215], v[52:55]\n"
      "v_accvgpr_write_b32 a71, v78\n"
      "v_cvt_pk_bf16_f32 v80, v80, v81\n"
      "v_mfma_f32_16x16x32_bf16 v[56:59], v[184:187], a[216:219], v[56:59]\n"
      "v_cvt_pk_bf16_f32 v82, v82, v83\n"
      "v_cvt_pk_bf16_f32 v84, v84, v85\n"
      "v_mfma_f32_16x16x32_bf16 v[60:63], v[184:187], a[220:223], v[60:63]\n"
      "v_cvt_pk_bf16_f32 v86, v86, v87\n"
      "v_accvgpr_write_b32 a72, v80\n"
      "v_mfma_f32_16x16x32_bf16 v[204:207], a[0:3], a[64:67], v[192:195]\n"
      "v_accvgpr_write_b32 a73, v82\n"
      "v_accvgpr_write_b32 a74, v84\n"
      "v_accvgpr_write_b32 a75, v86\n"
      "v_mfma_f32_16x16x32_bf16 v[204:207], a[4:7], a[68:71], v[204:207]\n"
      "v_cvt_pk_bf16_f32 v88, v88, v89\n"
      "v_cvt_pk_bf16_f32 v90, v90, v91\n"
      "v_cvt_pk_bf16_f32 v92, v92, v93\n"
      "v_cvt_pk_bf16_f32 v94, v94, v95\n"
      "v_accvgpr_write_b32 a76, v88\n"
      "v_accvgpr_write_b32 a77, v90\n"
      "v_accvgpr_write_b32 a78, v92\n"
      "v_accvgpr_write_b32 a79, v94\n"
      "v_mfma_f32_16x16x32_bf16 v[204:207], a[8:11], a[72:75], v[204:207]\n"
      "v_mfma_f32_16x16x32_bf16 v[204:207], a[12:15], a[76:79], v[204:207]\n"
      "buffer_load_dwordx4 v248, s[36:39], s59 offen offset:0 nt lds\n"
      "v_mfma_f32_16x16x32_bf16 v[164:167], a[160:163], a[64:67], v[204:207]\n"
      "buffer_load_dwordx4 v248, s[36:39], s59 offen offset:1024 nt lds\n"
      "v_mfma_f32_16x16x32_bf16 v[164:167], a[164:167], a[68:71], v[164:167]\n"
      "buffer_load_dwordx4 v249, s[36:39], s59 offen offset:2048 nt lds\n"
      "v_mfma_f32_16x16x32_bf16 v[164:167], a[168:171], a[72:75], v[164:167]\n"
      "buffer_load_dwordx4 v249, s[36:39], s59 offen offset:3072 nt lds\n"
      "v_mfma_f32_16x16x32_bf16 v[164:167], a[172:175], a[76:79], v[164:167]\n"
      "s_add_u32 m0, s59, 65536\n"
      "v_mfma_f32_16x16x32_bf16 v[160:163], a[188:191], a[76:79], v[204:207]\n"
      "v_mfma_f32_16x16x32_bf16 v[160:163], a[184:187], a[72:75], v[160:163]\n"
      "v_mfma_f32_16x16x32_bf16 v[160:163], a[180:183], a[68:71], v[160:163]\n"
      "v_mfma_f32_16x16x32_bf16 v[160:163], a[176:179], a[64:67], v[160:163]\n"
      "v_exp_f32 v164, v164\n"
      "v_mfma_f32_16x16x32_bf16 v[168:171], a[128:131], a[64:67], v[204:207]\n"
      "v_exp_f32 v165, v165\n"
      "v_mfma_f32_16x16x32_bf16 v[168:171], a[132:135], a[68:71], v[168:171]\n"
      "v_exp_f32 v166, v166\n"
      "v_mfma_f32_16x16x32_bf16 v[168:171], a[136:139], a[72:75], v[168:171]\n"
      "v_exp_f32 v167, v167\n"
      "v_mfma_f32_16x16x32_bf16 v[168:171], a[140:143], a[76:79], v[168:171]\n"
      "v_exp_f32 v160, v160\n"
      "v_mfma_f32_16x16x32_bf16 v[172:175], a[156:159], a[76:79], v[204:207]\n"
      "v_exp_f32 v161, v161\n"
      "v_mfma_f32_16x16x32_bf16 v[172:175], a[152:155], a[72:75], v[172:175]\n"
      "v_exp_f32 v162, v162\n"
      "v_exp_f32 v163, v163\n"
      "v_mfma_f32_16x16x32_bf16 v[172:175], a[148:151], a[68:71], v[172:175]\n"
      "v_cvt_pk_bf16_f32 v164, v164, v165\n"
      "v_cvt_pk_bf16_f32 v165, v166, v167\n"
      "v_cvt_pk_bf16_f32 v166, v160, v161\n"
      "v_cvt_pk_bf16_f32 v167, v162, v163\n"
      "v_mfma_f32_16x16x32_bf16 v[172:175], a[144:147], a[64:67], v[172:175]\n"
      "s_waitcnt vmcnt(8)\n"
      "ds_read_b128 a[80:83], v252 offset:36864\n"
      "ds_read_b128 a[84:87], v252 offset:37120\n"
      "v_exp_f32 v168, v168\n"
      "v_mfma_f32_16x16x32_bf16 v[64:67], v[164:167], a[224:227], v[236:239]\n"
      "v_exp_f32 v169, v169\n"
      "v_mfma_f32_16x16x32_bf16 v[68:71], v[164:167], a[228:231], v[236:239]\n"
      "ds_read_b128 a[88:91], v252 offset:37376\n"
      "ds_read_b128 a[92:95], v252 offset:37632\n"
      "v_cvt_pk_bf16_f32 v168, v168, v169\n"
      "v_mfma_f32_16x16x32_bf16 v[72:75], v[164:167], a[232:235], v[236:239]\n"
      "v_exp_f32 v170, v170\n"
      "v_mfma_f32_16x16x32_bf16 v[76:79], v[164:167], a[236:239], v[236:239]\n"
      "v_exp_f32 v171, v171\n"
      "v_mfma_f32_16x16x32_bf16 v[80:83], v[164:167], a[240:243], v[236:239]\n"
      "v_exp_f32 v172, v172\n"
      "v_mfma_f32_16x16x32_bf16 v[84:87], v[164:167], a[244:247], v[236:239]\n"
      "v_exp_f32 v173, v173\n"
      "v_mfma_f32_16x16x32_bf16 v[88:91], v[164:167], a[248:251], v[236:239]\n"
      "v_exp_f32 v174, v174\n"
      "v_mfma_f32_16x16x32_bf16 v[92:95], v[164:167], a[252:255], v[236:239]\n"
      "s_waitcnt lgkmcnt(2)\n"
      "v_exp_f32 v175, v175\n"
      "v_smfmac_f32_16x16x64_bf16 v[96:99], a[16:19], a[80:87], v245\n"
      "v_cvt_pk_bf16_f32 v169, v170, v171\n"
      "v_smfmac_f32_16x16x64_bf16 v[100:103], a[20:23], a[80:87], v245\n"
      "v_cvt_pk_bf16_f32 v170, v172, v173\n"
      "v_smfmac_f32_16x16x64_bf16 v[104:107], a[24:27], a[80:87], v245\n"
      "v_cvt_pk_bf16_f32 v171, v174, v175\n"
      "v_smfmac_f32_16x16x64_bf16 v[108:111], a[28:31], a[80:87], v245\n"
      "s_waitcnt lgkmcnt(0)\n"
      "v_smfmac_f32_16x16x64_bf16 v[112:115], a[16:19], a[88:95], v245\n"
      "v_cvt_pk_bf16_f32 v96, v96, v97\n"
      "v_smfmac_f32_16x16x64_bf16 v[116:119], a[20:23], a[88:95], v245\n"
      "v_cvt_pk_bf16_f32 v98, v98, v99\n"
      "v_smfmac_f32_16x16x64_bf16 v[120:123], a[24:27], a[88:95], v245\n"
      "v_cvt_pk_bf16_f32 v100, v100, v101\n"
      "v_smfmac_f32_16x16x64_bf16 v[124:127], a[28:31], a[88:95], v245\n"
      "v_cvt_pk_bf16_f32 v102, v102, v103\n"
      "v_smfmac_f32_16x16x64_bf16 v[224:227], v[240:243], v[164:171], v244\n"
      "v_accvgpr_write_b32 a80, v96\n"
      "v_mfma_f32_16x16x32_bf16 v[64:67], v[168:171], a[192:195], v[64:67]\n"
      "v_accvgpr_write_b32 a81, v98\n"
      "v_accvgpr_write_b32 a82, v100\n"
      "v_mfma_f32_16x16x32_bf16 v[68:71], v[168:171], a[196:199], v[68:71]\n"
      "v_accvgpr_write_b32 a83, v102\n"
      "v_cvt_pk_bf16_f32 v104, v104, v105\n"
      "v_mfma_f32_16x16x32_bf16 v[72:75], v[168:171], a[200:203], v[72:75]\n"
      "v_cvt_pk_bf16_f32 v106, v106, v107\n"
      "v_cvt_pk_bf16_f32 v108, v108, v109\n"
      "v_mfma_f32_16x16x32_bf16 v[76:79], v[168:171], a[204:207], v[76:79]\n"
      "v_cvt_pk_bf16_f32 v110, v110, v111\n"
      "v_accvgpr_write_b32 a84, v104\n"
      "v_mfma_f32_16x16x32_bf16 v[80:83], v[168:171], a[208:211], v[80:83]\n"
      "v_accvgpr_write_b32 a85, v106\n"
      "v_accvgpr_write_b32 a86, v108\n"
      "v_mfma_f32_16x16x32_bf16 v[84:87], v[168:171], a[212:215], v[84:87]\n"
      "v_accvgpr_write_b32 a87, v110\n"
      "v_cvt_pk_bf16_f32 v112, v112, v113\n"
      "v_mfma_f32_16x16x32_bf16 v[88:91], v[168:171], a[216:219], v[88:91]\n"
      "v_cvt_pk_bf16_f32 v114, v114, v115\n"
      "v_cvt_pk_bf16_f32 v116, v116, v117\n"
      "v_mfma_f32_16x16x32_bf16 v[92:95], v[168:171], a[220:223], v[92:95]\n"
      "v_cvt_pk_bf16_f32 v118, v118, v119\n"
      "v_accvgpr_write_b32 a88, v112\n"
      "v_mfma_f32_16x16x32_bf16 v[208:211], a[0:3], a[80:83], v[192:195]\n"
      "v_accvgpr_write_b32 a89, v114\n"
      "v_accvgpr_write_b32 a90, v116\n"
      "v_accvgpr_write_b32 a91, v118\n"
      "v_mfma_f32_16x16x32_bf16 v[208:211], a[4:7], a[84:87], v[208:211]\n"
      "v_cvt_pk_bf16_f32 v120, v120, v121\n"
      "v_cvt_pk_bf16_f32 v122, v122, v123\n"
      "v_cvt_pk_bf16_f32 v124, v124, v125\n"
      "v_cvt_pk_bf16_f32 v126, v126, v127\n"
      "v_accvgpr_write_b32 a92, v120\n"
      "v_accvgpr_write_b32 a93, v122\n"
      "v_accvgpr_write_b32 a94, v124\n"
      "v_accvgpr_write_b32 a95, v126\n"
      "v_mfma_f32_16x16x32_bf16 v[208:211], a[8:11], a[88:91], v[208:211]\n"
      "v_mfma_f32_16x16x32_bf16 v[208:211], a[12:15], a[92:95], v[208:211]\n"
      "buffer_load_dwordx4 v248, s[40:43], s59 offen offset:0 nt lds\n"
      "v_mfma_f32_16x16x32_bf16 v[180:183], a[160:163], a[80:83], v[208:211]\n"
      "buffer_load_dwordx4 v248, s[40:43], s59 offen offset:1024 nt lds\n"
      "v_mfma_f32_16x16x32_bf16 v[180:183], a[164:167], a[84:87], v[180:183]\n"
      "buffer_load_dwordx4 v248, s[40:43], s59 offen offset:2048 nt lds\n"
      "v_mfma_f32_16x16x32_bf16 v[180:183], a[168:171], a[88:91], v[180:183]\n"
      "buffer_load_dwordx4 v248, s[40:43], s59 offen offset:3072 nt lds\n"
      "v_mfma_f32_16x16x32_bf16 v[180:183], a[172:175], a[92:95], v[180:183]\n"
      "s_mov_b32 m0, s55\n"
      "v_mfma_f32_16x16x32_bf16 v[176:179], a[188:191], a[92:95], v[208:211]\n"
      "v_mfma_f32_16x16x32_bf16 v[176:179], a[184:187], a[88:91], v[176:179]\n"
      "v_mfma_f32_16x16x32_bf16 v[176:179], a[180:183], a[84:87], v[176:179]\n"
      "v_mfma_f32_16x16x32_bf16 v[176:179], a[176:179], a[80:83], v[176:179]\n"
      "v_exp_f32 v180, v180\n"
      "v_mfma_f32_16x16x32_bf16 v[184:187], a[128:131], a[80:83], v[208:211]\n"
      "v_exp_f32 v181, v181\n"
      "v_mfma_f32_16x16x32_bf16 v[184:187], a[132:135], a[84:87], v[184:187]\n"
      "v_exp_f32 v182, v182\n"
      "v_mfma_f32_16x16x32_bf16 v[184:187], a[136:139], a[88:91], v[184:187]\n"
      "v_exp_f32 v183, v183\n"
      "v_mfma_f32_16x16x32_bf16 v[184:187], a[140:143], a[92:95], v[184:187]\n"
      "v_exp_f32 v176, v176\n"
      "v_mfma_f32_16x16x32_bf16 v[188:191], a[156:159], a[92:95], v[208:211]\n"
      "v_exp_f32 v177, v177\n"
      "v_mfma_f32_16x16x32_bf16 v[188:191], a[152:155], a[88:91], v[188:191]\n"
      "v_exp_f32 v178, v178\n"
      "v_exp_f32 v179, v179\n"
      "v_mfma_f32_16x16x32_bf16 v[188:191], a[148:151], a[84:87], v[188:191]\n"
      "v_cvt_pk_bf16_f32 v180, v180, v181\n"
      "v_cvt_pk_bf16_f32 v181, v182, v183\n"
      "v_cvt_pk_bf16_f32 v182, v176, v177\n"
      "v_cvt_pk_bf16_f32 v183, v178, v179\n"
      "v_mfma_f32_16x16x32_bf16 v[188:191], a[144:147], a[80:83], v[188:191]\n"
      "s_waitcnt vmcnt(8)\n"
      "ds_read_b128 a[96:99], v253 offset:4096\n"
      "ds_read_b128 a[100:103], v253 offset:4352\n"
      "v_exp_f32 v184, v184\n"
      "v_mfma_f32_16x16x32_bf16 v[96:99], v[180:183], a[224:227], v[236:239]\n"
      "v_exp_f32 v185, v185\n"
      "v_mfma_f32_16x16x32_bf16 v[100:103], v[180:183], a[228:231], v[236:239]\n"
      "ds_read_b128 a[104:107], v253 offset:4608\n"
      "ds_read_b128 a[108:111], v253 offset:4864\n"
      "v_cvt_pk_bf16_f32 v184, v184, v185\n"
      "v_mfma_f32_16x16x32_bf16 v[104:107], v[180:183], a[232:235], v[236:239]\n"
      "v_exp_f32 v186, v186\n"
      "v_mfma_f32_16x16x32_bf16 v[108:111], v[180:183], a[236:239], v[236:239]\n"
      "v_exp_f32 v187, v187\n"
      "v_mfma_f32_16x16x32_bf16 v[112:115], v[180:183], a[240:243], v[236:239]\n"
      "v_exp_f32 v188, v188\n"
      "v_mfma_f32_16x16x32_bf16 v[116:119], v[180:183], a[244:247], v[236:239]\n"
      "v_exp_f32 v189, v189\n"
      "v_mfma_f32_16x16x32_bf16 v[120:123], v[180:183], a[248:251], v[236:239]\n"
      "v_exp_f32 v190, v190\n"
      "v_mfma_f32_16x16x32_bf16 v[124:127], v[180:183], a[252:255], v[236:239]\n"
      "s_waitcnt lgkmcnt(2)\n"
      "v_exp_f32 v191, v191\n"
      "v_smfmac_f32_16x16x64_bf16 v[128:131], a[16:19], a[96:103], v245\n"
      "v_cvt_pk_bf16_f32 v185, v186, v187\n"
      "v_smfmac_f32_16x16x64_bf16 v[132:135], a[20:23], a[96:103], v245\n"
      "v_cvt_pk_bf16_f32 v186, v188, v189\n"
      "v_smfmac_f32_16x16x64_bf16 v[136:139], a[24:27], a[96:103], v245\n"
      "v_cvt_pk_bf16_f32 v187, v190, v191\n"
      "v_smfmac_f32_16x16x64_bf16 v[140:143], a[28:31], a[96:103], v245\n"
      "s_waitcnt lgkmcnt(0)\n"
      "v_smfmac_f32_16x16x64_bf16 v[144:147], a[16:19], a[104:111], v245\n"
      "v_cvt_pk_bf16_f32 v128, v128, v129\n"
      "v_smfmac_f32_16x16x64_bf16 v[148:151], a[20:23], a[104:111], v245\n"
      "v_cvt_pk_bf16_f32 v130, v130, v131\n"
      "v_smfmac_f32_16x16x64_bf16 v[152:155], a[24:27], a[104:111], v245\n"
      "v_cvt_pk_bf16_f32 v132, v132, v133\n"
      "v_smfmac_f32_16x16x64_bf16 v[156:159], a[28:31], a[104:111], v245\n"
      "v_cvt_pk_bf16_f32 v134, v134, v135\n"
      "v_smfmac_f32_16x16x64_bf16 v[228:231], v[240:243], v[180:187], v244\n"
      "v_accvgpr_write_b32 a96, v128\n"
      "v_mfma_f32_16x16x32_bf16 v[96:99], v[184:187], a[192:195], v[96:99]\n"
      "v_accvgpr_write_b32 a97, v130\n"
      "v_accvgpr_write_b32 a98, v132\n"
      "v_mfma_f32_16x16x32_bf16 v[100:103], v[184:187], a[196:199], v[100:103]\n"
      "v_accvgpr_write_b32 a99, v134\n"
      "v_cvt_pk_bf16_f32 v136, v136, v137\n"
      "v_mfma_f32_16x16x32_bf16 v[104:107], v[184:187], a[200:203], v[104:107]\n"
      "v_cvt_pk_bf16_f32 v138, v138, v139\n"
      "v_cvt_pk_bf16_f32 v140, v140, v141\n"
      "v_mfma_f32_16x16x32_bf16 v[108:111], v[184:187], a[204:207], v[108:111]\n"
      "v_cvt_pk_bf16_f32 v142, v142, v143\n"
      "v_accvgpr_write_b32 a100, v136\n"
      "v_mfma_f32_16x16x32_bf16 v[112:115], v[184:187], a[208:211], v[112:115]\n"
      "v_accvgpr_write_b32 a101, v138\n"
      "v_accvgpr_write_b32 a102, v140\n"
      "v_mfma_f32_16x16x32_bf16 v[116:119], v[184:187], a[212:215], v[116:119]\n"
      "v_accvgpr_write_b32 a103, v142\n"
      "v_cvt_pk_bf16_f32 v144, v144, v145\n"
      "v_mfma_f32_16x16x32_bf16 v[120:123], v[184:187], a[216:219], v[120:123]\n"
      "v_cvt_pk_bf16_f32 v146, v146, v147\n"
      "v_cvt_pk_bf16_f32 v148, v148, v149\n"
      "v_mfma_f32_16x16x32_bf16 v[124:127], v[184:187], a[220:223], v[124:127]\n"
      "v_cvt_pk_bf16_f32 v150, v150, v151\n"
      "v_accvgpr_write_b32 a104, v144\n"
      "v_mfma_f32_16x16x32_bf16 v[192:195], a[0:3], a[96:99], v[192:195]\n"
      "v_accvgpr_write_b32 a105, v146\n"
      "v_accvgpr_write_b32 a106, v148\n"
      "v_accvgpr_write_b32 a107, v150\n"
      "v_mfma_f32_16x16x32_bf16 v[192:195], a[4:7], a[100:103], v[192:195]\n"
      "v_cvt_pk_bf16_f32 v152, v152, v153\n"
      "v_cvt_pk_bf16_f32 v154, v154, v155\n"
      "v_cvt_pk_bf16_f32 v156, v156, v157\n"
      "v_cvt_pk_bf16_f32 v158, v158, v159\n"
      "v_accvgpr_write_b32 a108, v152\n"
      "v_accvgpr_write_b32 a109, v154\n"
      "v_accvgpr_write_b32 a110, v156\n"
      "v_accvgpr_write_b32 a111, v158\n"
      "v_mfma_f32_16x16x32_bf16 v[192:195], a[8:11], a[104:107], v[192:195]\n"
      "v_mfma_f32_16x16x32_bf16 v[192:195], a[12:15], a[108:111], v[192:195]\n"
      "buffer_load_dwordx4 v248, s[36:39], s53 offen offset:0 nt lds\n"
      "v_mfma_f32_16x16x32_bf16 v[164:167], a[160:163], a[96:99], v[192:195]\n"
      "buffer_load_dwordx4 v248, s[36:39], s53 offen offset:1024 nt lds\n"
      "v_mfma_f32_16x16x32_bf16 v[164:167], a[164:167], a[100:103], v[164:167]\n"
      "buffer_load_dwordx4 v249, s[36:39], s53 offen offset:2048 nt lds\n"
      "v_mfma_f32_16x16x32_bf16 v[164:167], a[168:171], a[104:107], v[164:167]\n"
      "buffer_load_dwordx4 v249, s[36:39], s53 offen offset:3072 nt lds\n"
      "v_mfma_f32_16x16x32_bf16 v[164:167], a[172:175], a[108:111], v[164:167]\n"
      "s_add_u32 m0, s55, 4096\n"
      "v_mfma_f32_16x16x32_bf16 v[160:163], a[188:191], a[108:111], v[192:195]\n"
      "v_mfma_f32_16x16x32_bf16 v[160:163], a[184:187], a[104:107], v[160:163]\n"
      "v_mfma_f32_16x16x32_bf16 v[160:163], a[180:183], a[100:103], v[160:163]\n"
      "v_mfma_f32_16x16x32_bf16 v[160:163], a[176:179], a[96:99], v[160:163]\n"
      "s_waitcnt vmcnt(8)\n"
      "s_barrier\n"
      "v_mfma_f32_16x16x32_bf16 v[168:171], a[128:131], a[96:99], v[192:195]\n"
      "buffer_load_dwordx4 v248, s[36:39], s54 offen offset:0 nt lds\n"
      "v_exp_f32 v164, v164\n"
      "v_mfma_f32_16x16x32_bf16 v[168:171], a[132:135], a[100:103], v[168:171]\n"
      "v_exp_f32 v165, v165\n"
      "v_mfma_f32_16x16x32_bf16 v[168:171], a[136:139], a[104:107], v[168:171]\n"
      "buffer_load_dwordx4 v248, s[36:39], s54 offen offset:1024 nt lds\n"
      "v_exp_f32 v166, v166\n"
      "v_mfma_f32_16x16x32_bf16 v[168:171], a[140:143], a[108:111], v[168:171]\n"
      "v_exp_f32 v167, v167\n"
      "v_mfma_f32_16x16x32_bf16 v[172:175], a[156:159], a[108:111], v[192:195]\n"
      "buffer_load_dwordx4 v249, s[36:39], s54 offen offset:2048 nt lds\n"
      "v_exp_f32 v160, v160\n"
      "v_mfma_f32_16x16x32_bf16 v[172:175], a[152:155], a[104:107], v[172:175]\n"
      "ds_read_b128 a[160:163], v246 offset:0\n"
      "v_cvt_pk_bf16_f32 v164, v164, v165\n"
      "v_exp_f32 v161, v161\n"
      "v_exp_f32 v162, v162\n"
      "v_exp_f32 v163, v163\n"
      "v_mfma_f32_16x16x32_bf16 v[172:175], a[148:151], a[100:103], v[172:175]\n"
      "ds_read_b128 a[164:167], v246 offset:256\n"
      "buffer_load_dwordx4 v249, s[36:39], s54 offen offset:3072 nt lds\n"
      "v_cvt_pk_bf16_f32 v165, v166, v167\n"
      "v_cvt_pk_bf16_f32 v166, v160, v161\n"
      "v_cvt_pk_bf16_f32 v167, v162, v163\n"
      "v_mfma_f32_16x16x32_bf16 v[172:175], a[144:147], a[96:99], v[172:175]\n"
      "s_add_u32 m0, s55, 65536\n"
      "ds_read_b128 a[168:171], v246 offset:512\n"
      "ds_read_b128 a[172:175], v246 offset:768\n"
      "v_mfma_f32_16x16x32_bf16 v[128:131], v[164:167], a[224:227], v[236:239]\n"
      "v_exp_f32 v168, v168\n"
      "v_mfma_f32_16x16x32_bf16 v[132:135], v[164:167], a[228:231], v[236:239]\n"
      "ds_read_b128 a[188:191], v246 offset:4864\n"
      "v_exp_f32 v169, v169\n"
      "v_mfma_f32_16x16x32_bf16 v[136:139], v[164:167], a[232:235], v[236:239]\n"
      "ds_read_b128 a[128:131], v250 offset:16384\n"
      "ds_read_b128 a[184:187], v246 offset:4608\n"
      "v_cvt_pk_bf16_f32 v168, v168, v169\n"
      "v_mfma_f32_16x16x32_bf16 v[140:143], v[164:167], a[236:239], v[236:239]\n"
      "ds_read_b128 a[180:183], v246 offset:4352\n"
      "v_exp_f32 v170, v170\n"
      "v_mfma_f32_16x16x32_bf16 v[144:147], v[164:167], a[240:243], v[236:239]\n"
      "ds_read_b128 a[176:179], v246 offset:4096\n"
      "v_xor_b32 v246, 0x8000, v246\n"
      "v_mfma_f32_16x16x32_bf16 v[148:151], v[164:167], a[244:247], v[236:239]\n"
      "v_exp_f32 v171, v171\n"
      "v_mfma_f32_16x16x32_bf16 v[152:155], v[164:167], a[248:251], v[236:239]\n"
      "ds_read_b128 a[132:135], v250 offset:16640\n"
      "v_cvt_pk_bf16_f32 v169, v170, v171\n"
      "v_mfma_f32_16x16x32_bf16 v[156:159], v[164:167], a[252:255], v[236:239]\n"
      "s_waitcnt lgkmcnt(6)\n"
      "ds_read_b128 a[136:139], v250 offset:16896\n"
      "ds_read_b128 a[140:143], v250 offset:17152\n"
      "ds_read_b128 a[144:147], v250 offset:20480\n"
      "ds_read_b128 a[148:151], v250 offset:20736\n"
      "ds_read_b128 a[152:155], v250 offset:20992\n"
      "ds_read_b128 a[156:159], v250 offset:21248\n"
      "s_waitcnt vmcnt(8)\n"
      "s_barrier\n"
      "v_mfma_f32_16x16x32_bf16 v[176:179], a[160:163], a[112:115], v[212:215]\n"
      "buffer_load_dwordx4 v248, s[40:43], s53 offen offset:0 nt lds\n"
      "v_exp_f32 v172, v172\n"
      "v_mfma_f32_16x16x32_bf16 v[176:179], a[164:167], a[116:119], v[176:179]\n"
      "v_exp_f32 v173, v173\n"
      "v_mfma_f32_16x16x32_bf16 v[176:179], a[168:171], a[120:123], v[176:179]\n"
      "ds_read_b64_tr_b16 a[224:225], v247 offset:0\n"
      "buffer_load_dwordx4 v248, s[40:43], s53 offen offset:1024 nt lds\n"
      "v_cvt_pk_bf16_f32 v170, v172, v173\n"
      "v_mfma_f32_16x16x32_bf16 v[176:179], a[172:175], a[124:127], v[176:179]\n"
      "s_waitcnt lgkmcnt(9)\n"
      "v_exp_f32 v174, v174\n"
      "v_mfma_f32_16x16x32_bf16 v[180:183], a[188:191], a[124:127], v[212:215]\n"
      "buffer_load_dwordx4 v248, s[40:43], s53 offen offset:2048 nt lds\n"
      "v_exp_f32 v175, v175\n"
      "v_mfma_f32_16x16x32_bf16 v[180:183], a[184:187], a[120:123], v[180:183]\n"
      "ds_read_b64_tr_b16 a[226:227], v247 offset:4096\n"
      "v_cvt_pk_bf16_f32 v171, v174, v175\n"
      "v_mfma_f32_16x16x32_bf16 v[180:183], a[180:183], a[116:119], v[180:183]\n"
      "s_waitcnt lgkmcnt(8)\n"
      "ds_read_b64_tr_b16 a[228:229], v247 offset:8\n"
      "ds_read_b64_tr_b16 a[230:231], v247 offset:4104\n"
      "buffer_load_dwordx4 v248, s[40:43], s53 offen offset:3072 nt lds\n"
      "v_mfma_f32_16x16x32_bf16 v[180:183], a[176:179], a[112:115], v[180:183]\n"
      "s_add_u32 m0, s55, 69632\n"
      "v_exp_f32 v176, v176\n"
      "v_smfmac_f32_16x16x64_bf16 v[232:235], v[240:243], v[164:171], v244\n"
      "s_add_u32 s53, s53, 32768\n"
      "ds_read_b128 a[160:163], v250 offset:24576\n"
      "buffer_load_dwordx4 v248, s[40:43], s54 offen offset:0 nt lds\n"
      "v_mfma_f32_16x16x32_bf16 v[128:131], v[168:171], a[192:195], v[128:131]\n"
      "v_exp_f32 v177, v177\n"
      "v_mfma_f32_16x16x32_bf16 v[132:135], v[168:171], a[196:199], v[132:135]\n"
      "ds_read_b64_tr_b16 a[232:233], v247 offset:256\n"
      "buffer_load_dwordx4 v248, s[40:43], s54 offen offset:1024 nt lds\n"
      "v_cvt_pk_bf16_f32 v176, v176, v177\n"
      "v_mfma_f32_16x16x32_bf16 v[136:139], v[168:171], a[200:203], v[136:139]\n"
      "v_exp_f32 v178, v178\n"
      "v_mfma_f32_16x16x32_bf16 v[140:143], v[168:171], a[204:207], v[140:143]\n"
      "buffer_load_dwordx4 v248, s[40:43], s54 offen offset:2048 nt lds\n"
      "v_exp_f32 v179, v179\n"
      "v_mfma_f32_16x16x32_bf16 v[144:147], v[168:171], a[208:211], v[144:147]\n"
      "ds_read_b64_tr_b16 a[234:235], v247 offset:4352\n"
      "v_cvt_pk_bf16_f32 v177, v178, v179\n"
      "v_mfma_f32_16x16x32_bf16 v[148:151], v[168:171], a[212:215], v[148:151]\n"
      "buffer_load_dwordx4 v248, s[40:43], s54 offen offset:3072 nt lds\n"
      "v_exp_f32 v180, v180\n"
      "v_mfma_f32_16x16x32_bf16 v[152:155], v[168:171], a[216:219], v[152:155]\n"
      "s_add_u32 s54, s54, 32768\n"
      "v_exp_f32 v181, v181\n"
      "v_mfma_f32_16x16x32_bf16 v[156:159], v[168:171], a[220:223], v[156:159]\n"
      "s_xor_b32 s55, s55, 0x8000\n"
      "ds_read_b64_tr_b16 a[192:193], v251 offset:16384\n"
      "v_cvt_pk_bf16_f32 v178, v180, v181\n"
      "v_mfma_f32_16x16x32_bf16 v[184:187], a[128:131], a[32:35], v[196:199]\n"
      "v_exp_f32 v182, v182\n"
      "v_mfma_f32_16x16x32_bf16 v[184:187], a[132:135], a[36:39], v[184:187]\n"
      "s_waitcnt lgkmcnt(2)\n"
      "v_exp_f32 v183, v183\n"
      "v_mfma_f32_16x16x32_bf16 v[184:187], a[136:139], a[40:43], v[184:187]\n"
      "ds_read_b64_tr_b16 a[236:237], v247 offset:264\n"
      "v_cvt_pk_bf16_f32 v179, v182, v183\n"
      "v_mfma_f32_16x16x32_bf16 v[184:187], a[140:143], a[44:47], v[184:187]\n"
      "ds_read_b64_tr_b16 a[194:195], v251 offset:20480\n"
      "ds_read_b128 a[164:167], v250 offset:24832\n"
      "v_mfma_f32_16x16x32_bf16 v[188:191], a[156:159], a[44:47], v[196:199]\n"
      "ds_read_b64_tr_b16 a[238:239], v247 offset:4360\n"
      "ds_read_b64_tr_b16 a[196:197], v251 offset:16392\n"
      "v_mfma_f32_16x16x32_bf16 v[188:191], a[152:155], a[40:43], v[188:191]\n"
      "ds_read_b64_tr_b16 a[240:241], v247 offset:512\n"
      "ds_read_b64_tr_b16 a[198:199], v251 offset:20488\n"
      "v_mfma_f32_16x16x32_bf16 v[188:191], a[148:151], a[36:39], v[188:191]\n"
      "ds_read_b64_tr_b16 a[242:243], v247 offset:4608\n"
      "ds_read_b64_tr_b16 a[200:201], v251 offset:16640\n"
      "v_mfma_f32_16x16x32_bf16 v[188:191], a[144:147], a[32:35], v[188:191]\n"
      "v_exp_f32 v184, v184\n"
      "v_mfma_f32_16x16x32_bf16 v[160:163], a[128:131], a[48:51], v[200:203]\n"
      "ds_read_b128 a[168:171], v250 offset:25088\n"
      "v_add_f32 v216, v216, v184\n"
      "v_mfma_f32_16x16x32_bf16 v[160:163], a[132:135], a[52:55], v[160:163]\n"
      "v_exp_f32 v185, v185\n"
      "v_mfma_f32_16x16x32_bf16 v[160:163], a[136:139], a[56:59], v[160:163]\n"
      "ds_read_b64_tr_b16 a[244:245], v247 offset:520\n"
      "v_add_f32 v216, v216, v185\n"
      "v_mfma_f32_16x16x32_bf16 v[160:163], a[140:143], a[60:63], v[160:163]\n"
      "ds_read_b64_tr_b16 a[202:203], v251 offset:20736\n"
      "v_cvt_pk_bf16_f32 v184, v184, v185\n"
      "v_mfma_f32_16x16x32_bf16 v[164:167], a[156:159], a[60:63], v[200:203]\n"
      "v_exp_f32 v186, v186\n"
      "v_mfma_f32_16x16x32_bf16 v[164:167], a[152:155], a[56:59], v[164:167]\n"
      "ds_read_b64_tr_b16 a[246:247], v247 offset:4616\n"
      "v_add_f32 v216, v216, v186\n"
      "v_mfma_f32_16x16x32_bf16 v[164:167], a[148:151], a[52:55], v[164:167]\n"
      "v_exp_f32 v187, v187\n"
      "v_mfma_f32_16x16x32_bf16 v[164:167], a[144:147], a[48:51], v[164:167]\n"
      "ds_read_b64_tr_b16 a[204:205], v251 offset:16648\n"
      "v_add_f32 v216, v216, v187\n"
      "v_mfma_f32_16x16x32_bf16 v[168:171], a[128:131], a[64:67], v[204:207]\n"
      "ds_read_b64_tr_b16 a[248:249], v247 offset:768\n"
      "v_cvt_pk_bf16_f32 v185, v186, v187\n"
      "v_mfma_f32_16x16x32_bf16 v[168:171], a[132:135], a[68:71], v[168:171]\n"
      "v_exp_f32 v188, v188\n"
      "v_mfma_f32_16x16x32_bf16 v[168:171], a[136:139], a[72:75], v[168:171]\n"
      "ds_read_b64_tr_b16 a[206:207], v251 offset:20744\n"
      "v_add_f32 v216, v216, v188\n"
      "v_mfma_f32_16x16x32_bf16 v[168:171], a[140:143], a[76:79], v[168:171]\n"
      "v_exp_f32 v189, v189\n"
      "v_mfma_f32_16x16x32_bf16 v[172:175], a[156:159], a[76:79], v[204:207]\n"
      "ds_read_b64_tr_b16 a[208:209], v251 offset:16896\n"
      "ds_read_b64_tr_b16 a[250:251], v247 offset:4864\n"
      "v_cvt_pk_bf16_f32 v186, v188, v189\n"
      "v_mfma_f32_16x16x32_bf16 v[172:175], a[152:155], a[72:75], v[172:175]\n"
      "ds_read_b128 a[172:175], v250 offset:25344\n"
      "ds_read_b64_tr_b16 a[252:253], v247 offset:776\n"
      "ds_read_b64_tr_b16 a[254:255], v247 offset:4872\n"
      "v_add_f32 v216, v216, v189\n"
      "v_mfma_f32_16x16x32_bf16 v[172:175], a[148:151], a[68:71], v[172:175]\n"
      "ds_read_b64_tr_b16 a[210:211], v251 offset:20992\n"
      "v_xor_b32 v247, 0x8000, v247\n"
      "v_mfma_f32_16x16x32_bf16 v[172:175], a[144:147], a[64:67], v[172:175]\n"
      "v_exp_f32 v190, v190\n"
      "v_mfma_f32_16x16x32_bf16 v[236:239], v[240:243], v[176:179], 0\n"
      "ds_read_b64_tr_b16 a[212:213], v251 offset:16904\n"
      "v_add_f32 v216, v216, v190\n"
      "v_mfma_f32_16x16x32_bf16 a[0:3], a[224:227], v[176:179], 0\n"
      "v_exp_f32 v191, v191\n"
      "v_mfma_f32_16x16x32_bf16 a[4:7], a[228:231], v[176:179], 0\n"
      "s_waitcnt lgkmcnt(7)\n"
      "ds_read_b64_tr_b16 a[214:215], v251 offset:21000\n"
      "v_cvt_pk_bf16_f32 v187, v190, v191\n"
      "v_mfma_f32_16x16x32_bf16 a[8:11], a[232:235], v[176:179], 0\n"
      ".long 0xBEE500FF\n"
      "PAD1%=:\n"
      "s_setpc_b64 s[94:95]\n"
      "ds_read_b128 a[176:179], v250 offset:28672\n"
      "v_add_f32 v216, v216, v191\n"
      "v_mfma_f32_16x16x32_bf16 a[12:15], a[236:239], v[176:179], 0\n"
      "ds_read_b64_tr_b16 a[216:217], v251 offset:17152\n"
      "ds_read_b64_tr_b16 a[218:219], v251 offset:21248\n"
      "v_mfma_f32_16x16x32_bf16 a[16:19], a[240:243], v[176:179], 0\n"
      "ds_read_b64_tr_b16 a[220:221], v251 offset:17160\n"
      "ds_read_b128 a[180:183], v250 offset:28928\n"
      "ds_read_b64_tr_b16 a[222:223], v251 offset:21256\n"
      "v_mfma_f32_16x16x32_bf16 a[20:23], a[244:247], v[176:179], 0\n"
      "s_waitcnt lgkmcnt(8)\n"
      "v_exp_f32 v160, v160\n"
      "v_mfma_f32_16x16x32_bf16 a[24:27], a[248:251], v[176:179], 0\n"
      "ds_read_b128 a[184:187], v250 offset:29184\n"
      "v_add_f32 v220, v220, v160\n"
      "v_mfma_f32_16x16x32_bf16 a[28:31], a[252:255], v[176:179], 0\n"
      "v_exp_f32 v161, v161\n"
      "v_mfma_f32_16x16x32_bf16 v[0:3], v[184:187], a[192:195], v[0:3]\n"
      "ds_read_b64_tr_b16 a[224:225], v251 offset:24576\n"
      "v_add_f32 v220, v220, v161\n"
      "v_mfma_f32_16x16x32_bf16 v[4:7], v[184:187], a[196:199], v[4:7]\n"
      "ds_read_b128 a[188:191], v250 offset:29440\n"
      "v_cvt_pk_bf16_f32 v160, v160, v161\n"
      "v_mfma_f32_16x16x32_bf16 v[8:11], v[184:187], a[200:203], v[8:11]\n"
      "v_exp_f32 v162, v162\n"
      "v_mfma_f32_16x16x32_bf16 v[12:15], v[184:187], a[204:207], v[12:15]\n"
      "ds_read_b64_tr_b16 a[226:227], v251 offset:28672\n"
      "v_add_f32 v220, v220, v162\n"
      "v_mfma_f32_16x16x32_bf16 v[16:19], v[184:187], a[208:211], v[16:19]\n"
      "s_waitcnt lgkmcnt(4)\n"
      "v_exp_f32 v163, v163\n"
      "v_mfma_f32_16x16x32_bf16 v[20:23], v[184:187], a[212:215], v[20:23]\n"
      "ds_read_b64_tr_b16 a[228:229], v251 offset:24584\n"
      "v_add_f32 v220, v220, v163\n"
      "v_mfma_f32_16x16x32_bf16 v[24:27], v[184:187], a[216:219], v[24:27]\n"
      "ds_read_b64_tr_b16 a[230:231], v251 offset:28680\n"
      "v_cvt_pk_bf16_f32 v161, v162, v163\n"
      "v_mfma_f32_16x16x32_bf16 v[28:31], v[184:187], a[220:223], v[28:31]\n"
      "v_exp_f32 v164, v164\n"
      "v_mfma_f32_16x16x32_bf16 v[180:183], a[128:131], a[80:83], v[208:211]\n"
      "ds_read_b64_tr_b16 a[232:233], v251 offset:24832\n"
      "v_add_f32 v220, v220, v164\n"
      "v_mfma_f32_16x16x32_bf16 v[180:183], a[132:135], a[84:87], v[180:183]\n"
      "v_exp_f32 v168, v168\n"
      "v_mfma_f32_16x16x32_bf16 v[180:183], a[136:139], a[88:91], v[180:183]\n"
      "ds_read_b64_tr_b16 a[234:235], v251 offset:28928\n"
      "v_add_f32 v224, v224, v168\n"
      "v_mfma_f32_16x16x32_bf16 v[180:183], a[140:143], a[92:95], v[180:183]\n"
      "v_exp_f32 v165, v165\n"
      "v_mfma_f32_16x16x32_bf16 v[176:179], a[156:159], a[92:95], v[208:211]\n"
      "ds_read_b64_tr_b16 a[236:237], v251 offset:24840\n"
      "v_cvt_pk_bf16_f32 v162, v164, v165\n"
      "v_mfma_f32_16x16x32_bf16 v[176:179], a[152:155], a[88:91], v[176:179]\n"
      "v_exp_f32 v169, v169\n"
      "v_mfma_f32_16x16x32_bf16 v[176:179], a[148:151], a[84:87], v[176:179]\n"
      "ds_read_b64_tr_b16 a[238:239], v251 offset:28936\n"
      "v_add_f32 v224, v224, v169\n"
      "v_mfma_f32_16x16x32_bf16 v[176:179], a[144:147], a[80:83], v[176:179]\n"
      "v_exp_f32 v166, v166\n"
      "v_mfma_f32_16x16x32_bf16 v[184:187], a[160:163], a[80:83], v[208:211]\n"
      "v_exp_f32 v170, v170\n"
      "v_mfma_f32_16x16x32_bf16 v[184:187], a[164:167], a[84:87], v[184:187]\n"
      "v_exp_f32 v167, v167\n"
      "v_mfma_f32_16x16x32_bf16 v[184:187], a[168:171], a[88:91], v[184:187]\n"
      "v_exp_f32 v171, v171\n"
      "v_mfma_f32_16x16x32_bf16 v[184:187], a[172:175], a[92:95], v[184:187]\n"
      "s_waitcnt lgkmcnt(2)\n"
      "ds_read_b64_tr_b16 a[240:241], v251 offset:25088\n"
      "v_cvt_pk_bf16_f32 v163, v166, v167\n"
      "v_mfma_f32_16x16x32_bf16 v[188:191], a[188:191], a[92:95], v[208:211]\n"
      "v_exp_f32 v172, v172\n"
      "v_mfma_f32_16x16x32_bf16 v[188:191], a[184:187], a[88:91], v[188:191]\n"
      "ds_read_b64_tr_b16 a[242:243], v251 offset:29184\n"
      "v_add_f32 v224, v224, v170\n"
      "v_mfma_f32_16x16x32_bf16 v[188:191], a[180:183], a[84:87], v[188:191]\n"
      "v_exp_f32 v173, v173\n"
      "v_mfma_f32_16x16x32_bf16 v[188:191], a[176:179], a[80:83], v[188:191]\n"
      "v_exp_f32 v174, v174\n"
      "v_mfma_f32_16x16x32_bf16 v[32:35], v[160:163], a[192:195], v[32:35]\n"
      "ds_read_b64_tr_b16 a[244:245], v251 offset:25096\n"
      "v_add_f32 v224, v224, v171\n"
      "v_mfma_f32_16x16x32_bf16 v[36:39], v[160:163], a[196:199], v[36:39]\n"
      "v_exp_f32 v175, v175\n"
      "v_mfma_f32_16x16x32_bf16 v[40:43], v[160:163], a[200:203], v[40:43]\n"
      "ds_read_b64_tr_b16 a[246:247], v251 offset:29192\n"
      "v_cvt_pk_bf16_f32 v168, v168, v169\n"
      "v_mfma_f32_16x16x32_bf16 v[44:47], v[160:163], a[204:207], v[44:47]\n"
      "v_cvt_pk_bf16_f32 v169, v170, v171\n"
      "v_cvt_pk_bf16_f32 v170, v172, v173\n"
      "v_mfma_f32_16x16x32_bf16 v[48:51], v[160:163], a[208:211], v[48:51]\n"
      "v_cvt_pk_bf16_f32 v171, v174, v175\n"
      "v_add_f32 v220, v220, v165\n"
      "v_mfma_f32_16x16x32_bf16 v[52:55], v[160:163], a[212:215], v[52:55]\n"
      "v_add_f32 v220, v220, v166\n"
      "v_add_f32 v220, v220, v167\n"
      "v_mfma_f32_16x16x32_bf16 v[56:59], v[160:163], a[216:219], v[56:59]\n"
      "ds_read_b64_tr_b16 a[248:249], v251 offset:25344\n"
      "v_add_f32 v224, v224, v172\n"
      "v_mfma_f32_16x16x32_bf16 v[60:63], v[160:163], a[220:223], v[60:63]\n"
      "v_exp_f32 v180, v180\n"
      "v_mfma_f32_16x16x32_bf16 v[64:67], v[168:171], a[192:195], v[64:67]\n"
      "v_exp_f32 v184, v184\n"
      "v_mfma_f32_16x16x32_bf16 v[68:71], v[168:171], a[196:199], v[68:71]\n"
      "ds_read_b64_tr_b16 a[250:251], v251 offset:29440\n"
      "v_add_f32 v224, v224, v173\n"
      "v_mfma_f32_16x16x32_bf16 v[72:75], v[168:171], a[200:203], v[72:75]\n"
      "v_exp_f32 v181, v181\n"
      "v_mfma_f32_16x16x32_bf16 v[76:79], v[168:171], a[204:207], v[76:79]\n"
      "v_exp_f32 v185, v185\n"
      "v_mfma_f32_16x16x32_bf16 v[80:83], v[168:171], a[208:211], v[80:83]\n"
      "ds_read_b64_tr_b16 a[252:253], v251 offset:25352\n"
      "v_add_f32 v224, v224, v174\n"
      "v_mfma_f32_16x16x32_bf16 v[84:87], v[168:171], a[212:215], v[84:87]\n"
      "v_exp_f32 v182, v182\n"
      "v_mfma_f32_16x16x32_bf16 v[88:91], v[168:171], a[216:219], v[88:91]\n"
      "v_exp_f32 v186, v186\n"
      "v_mfma_f32_16x16x32_bf16 v[92:95], v[168:171], a[220:223], v[92:95]\n"
      "ds_read_b64_tr_b16 a[254:255], v251 offset:29448\n"
      "v_add_f32 v224, v224, v175\n"
      "v_mfma_f32_16x16x32_bf16 v[164:167], a[128:131], a[96:99], v[192:195]\n"
      "v_exp_f32 v183, v183\n"
      "v_mfma_f32_16x16x32_bf16 v[164:167], a[132:135], a[100:103], v[164:167]\n"
      "v_exp_f32 v187, v187\n"
      "v_mfma_f32_16x16x32_bf16 v[164:167], a[136:139], a[104:107], v[164:167]\n"
      "v_exp_f32 v176, v176\n"
      "v_mfma_f32_16x16x32_bf16 v[164:167], a[140:143], a[108:111], v[164:167]\n"
      "v_exp_f32 v188, v188\n"
      "v_mfma_f32_16x16x32_bf16 v[160:163], a[156:159], a[108:111], v[192:195]\n"
      "v_exp_f32 v177, v177\n"
      "v_mfma_f32_16x16x32_bf16 v[160:163], a[152:155], a[104:107], v[160:163]\n"
      "v_exp_f32 v189, v189\n"
      "v_mfma_f32_16x16x32_bf16 v[160:163], a[148:151], a[100:103], v[160:163]\n"
      "v_exp_f32 v178, v178\n"
      "v_mfma_f32_16x16x32_bf16 v[160:163], a[144:147], a[96:99], v[160:163]\n"
      "v_exp_f32 v164, v164\n"
      "v_mfma_f32_16x16x32_bf16 v[168:171], a[160:163], a[96:99], v[192:195]\n"
      "v_exp_f32 v165, v165\n"
      "v_mfma_f32_16x16x32_bf16 v[168:171], a[164:167], a[100:103], v[168:171]\n"
      "v_exp_f32 v166, v166\n"
      "v_mfma_f32_16x16x32_bf16 v[168:171], a[168:171], a[104:107], v[168:171]\n"
      "v_exp_f32 v167, v167\n"
      "v_mfma_f32_16x16x32_bf16 v[168:171], a[172:175], a[108:111], v[168:171]\n"
      "v_exp_f32 v160, v160\n"
      "v_mfma_f32_16x16x32_bf16 v[172:175], a[188:191], a[108:111], v[192:195]\n"
      "v_exp_f32 v190, v190\n"
      "v_mfma_f32_16x16x32_bf16 v[172:175], a[184:187], a[104:107], v[172:175]\n"
      "v_exp_f32 v161, v161\n"
      "v_exp_f32 v179, v179\n"
      "v_exp_f32 v191, v191\n"
      "v_mfma_f32_16x16x32_bf16 v[172:175], a[180:183], a[100:103], v[172:175]\n"
      "v_exp_f32 v162, v162\n"
      "v_cvt_pk_bf16_f32 v180, v180, v181\n"
      "v_cvt_pk_bf16_f32 v181, v182, v183\n"
      "v_cvt_pk_bf16_f32 v182, v176, v177\n"
      "v_cvt_pk_bf16_f32 v183, v178, v179\n"
      "v_cvt_pk_bf16_f32 v184, v184, v185\n"
      "v_cvt_pk_bf16_f32 v185, v186, v187\n"
      "v_cvt_pk_bf16_f32 v186, v188, v189\n"
      "v_cvt_pk_bf16_f32 v187, v190, v191\n"
      "v_mfma_f32_16x16x32_bf16 v[172:175], a[176:179], a[96:99], v[172:175]\n"
      "v_exp_f32 v163, v163\n"
      "LOOP%=:\n"
      "v_smfmac_f32_16x16x64_bf16 v[228:231], v[240:243], v[180:187], v244\n"
      "s_waitcnt vmcnt(0) lgkmcnt(0)\n"
      "ds_add_u32 v254, v255\n"
      "v_add_f32 v232, v232, v164\n"
      "v_mfma_f32_16x16x32_bf16 v[96:99], v[180:183], a[192:195], v[96:99]\n"
      "v_exp_f32 v168, v168\n"
      "v_mfma_f32_16x16x32_bf16 v[96:99], v[184:187], a[224:227], v[96:99]\n"
      "v_exp_f32 v169, v169\n"
      "v_mfma_f32_16x16x32_bf16 v[100:103], v[184:187], a[228:231], v[100:103]\n"
      "v_exp_f32 v170, v170\n"
      "v_mfma_f32_16x16x32_bf16 v[100:103], v[180:183], a[196:199], v[100:103]\n"
      "v_exp_f32 v171, v171\n"
      "v_mfma_f32_16x16x32_bf16 v[104:107], v[180:183], a[200:203], v[104:107]\n"
      "v_exp_f32 v172, v172\n"
      "v_mfma_f32_16x16x32_bf16 v[104:107], v[184:187], a[232:235], v[104:107]\n"
      "v_exp_f32 v173, v173\n"
      "v_mfma_f32_16x16x32_bf16 v[108:111], v[184:187], a[236:239], v[108:111]\n"
      "ds_read_b32 v253, v254 offset:0\n"
      "v_add_f32 v232, v232, v165\n"
      "v_mfma_f32_16x16x32_bf16 v[108:111], v[180:183], a[204:207], v[108:111]\n"
      "v_exp_f32 v174, v174\n"
      "v_mfma_f32_16x16x32_bf16 v[112:115], v[180:183], a[208:211], v[112:115]\n"
      "v_add_f32 v232, v232, v166\n"
      "v_add_f32 v232, v232, v167\n"
      "v_mfma_f32_16x16x32_bf16 v[112:115], v[184:187], a[240:243], v[112:115]\n"
      "v_cvt_pk_bf16_f32 v164, v164, v165\n"
      "v_cvt_pk_bf16_f32 v165, v166, v167\n"
      "v_mfma_f32_16x16x32_bf16 v[116:119], v[184:187], a[244:247], v[116:119]\n"
      "v_cvt_pk_bf16_f32 v166, v160, v161\n"
      "v_cvt_pk_bf16_f32 v167, v162, v163\n"
      "v_mfma_f32_16x16x32_bf16 v[116:119], v[180:183], a[212:215], v[116:119]\n"
      "v_exp_f32 v175, v175\n"
      "v_mfma_f32_16x16x32_bf16 v[120:123], v[180:183], a[216:219], v[120:123]\n"
      "s_waitcnt lgkmcnt(0)\n"
      "v_cmp_gt_u32 vcc, s100, v253\n"
      "v_add_f32 v232, v232, v160\n"
      "v_mfma_f32_16x16x32_bf16 v[120:123], v[184:187], a[248:251], v[120:123]\n"
      "s_cbranch_vccnz SPINA0x%=\n"
      "BACKA0x%=:\n"
      "v_add_f32 v232, v232, v161\n"
      "v_add_f32 v232, v232, v162\n"
      "v_mfma_f32_16x16x32_bf16 v[124:127], v[184:187], a[252:255], v[124:127]\n"
      "s_addk_i32 s100, 4\n"
      "ds_read_b128 a[128:131], v246 offset:0\n"
      "v_add_f32 v232, v232, v163\n"
      "v_mfma_f32_16x16x32_bf16 v[124:127], v[180:183], a[220:223], v[124:127]\n"
      "s_add_u32 m0, s55, 0\n"
      "ds_read_b128 a[132:135], v246 offset:256\n"
      "v_add_f32 v232, v232, v168\n"
      "v_mfma_f32_16x16x32_bf16 v[176:179], a[160:163], a[64:67], v[204:207]\n"
      "v_add_f32 v232, v232, v169\n"
      "v_add_f32 v232, v232, v170\n"
      "v_mfma_f32_16x16x32_bf16 v[176:179], a[164:167], a[68:71], v[176:179]\n"
      "ds_read_b128 a[136:139], v246 offset:512\n"
      "v_add_f32 v232, v232, v171\n"
      "v_mfma_f32_16x16x32_bf16 v[176:179], a[168:171], a[72:75], v[176:179]\n"
      "v_cvt_pk_bf16_f32 v168, v168, v169\n"
      "v_cvt_pk_bf16_f32 v169, v170, v171\n"
      "v_mfma_f32_16x16x32_bf16 v[176:179], a[172:175], a[76:79], v[176:179]\n"
      "ds_read_b128 a[140:143], v246 offset:768\n"
      "v_cvt_pk_bf16_f32 v170, v172, v173\n"
      "v_mfma_f32_16x16x32_bf16 v[180:183], a[188:191], a[76:79], v[204:207]\n"
      "v_cvt_pk_bf16_f32 v171, v174, v175\n"
      "v_add_f32 v232, v232, v172\n"
      "v_mfma_f32_16x16x32_bf16 v[180:183], a[184:187], a[72:75], v[180:183]\n"
      "ds_read_b128 a[144:147], v246 offset:4096\n"
      "v_add_f32 v232, v232, v173\n"
      "v_mfma_f32_16x16x32_bf16 v[180:183], a[180:183], a[68:71], v[180:183]\n"
      "ds_read_b128 a[148:151], v246 offset:4352\n"
      "v_add_f32 v232, v232, v174\n"
      "v_mfma_f32_16x16x32_bf16 v[180:183], a[176:179], a[64:67], v[180:183]\n"
      "v_exp_f32 v176, v176\n"
      "v_mfma_f32_16x16x32_bf16 v[128:131], v[164:167], a[192:195], v[128:131]\n"
      "ds_read_b64_tr_b16 a[192:193], v247 offset:0\n"
      "v_add_f32 v232, v232, v175\n"
      "v_mfma_f32_16x16x32_bf16 v[128:131], v[168:171], a[224:227], v[128:131]\n"
      "ds_read_b64_tr_b16 a[194:195], v247 offset:4096\n"
      "v_add_f32 v224, v224, v176\n"
      "v_mfma_f32_16x16x32_bf16 v[132:135], v[168:171], a[228:231], v[132:135]\n"
      "v_exp_f32 v177, v177\n"
      "v_mfma_f32_16x16x32_bf16 v[132:135], v[164:167], a[196:199], v[132:135]\n"
      "buffer_load_dwordx4 v248, s[36:39], s53 offen offset:0 nt lds\n"
      "v_mfma_f32_16x16x32_bf16 v[136:139], v[164:167], a[200:203], v[136:139]\n"
      "ds_read_b128 a[152:155], v246 offset:4608\n"
      "v_add_f32 v224, v224, v177\n"
      "v_mfma_f32_16x16x32_bf16 v[136:139], v[168:171], a[232:235], v[136:139]\n"
      "ds_read_b64_tr_b16 a[196:197], v247 offset:8\n"
      "v_cvt_pk_bf16_f32 v176, v176, v177\n"
      "v_mfma_f32_16x16x32_bf16 v[140:143], v[168:171], a[236:239], v[140:143]\n"
      "v_exp_f32 v178, v178\n"
      "v_mfma_f32_16x16x32_bf16 v[140:143], v[164:167], a[204:207], v[140:143]\n"
      "ds_read_b128 a[156:159], v246 offset:4864\n"
      "v_xor_b32 v246, 0x2000, v246\n"
      "v_mfma_f32_16x16x32_bf16 v[144:147], v[164:167], a[208:211], v[144:147]\n"
      "ds_read_b64_tr_b16 a[198:199], v247 offset:4104\n"
      "v_add_f32 v224, v224, v178\n"
      "v_mfma_f32_16x16x32_bf16 v[144:147], v[168:171], a[240:243], v[144:147]\n"
      "v_exp_f32 v179, v179\n"
      "v_mfma_f32_16x16x32_bf16 v[148:151], v[168:171], a[244:247], v[148:151]\n"
      "ds_read_b64_tr_b16 a[200:201], v247 offset:256\n"
      "v_add_f32 v224, v224, v179\n"
      "v_mfma_f32_16x16x32_bf16 v[148:151], v[164:167], a[212:215], v[148:151]\n"
      "v_exp_f32 v180, v180\n"
      "v_mfma_f32_16x16x32_bf16 v[152:155], v[164:167], a[216:219], v[152:155]\n"
      "ds_read_b64_tr_b16 a[202:203], v247 offset:4352\n"
      "v_cvt_pk_bf16_f32 v177, v178, v179\n"
      "v_mfma_f32_16x16x32_bf16 v[152:155], v[168:171], a[248:251], v[152:155]\n"
      "v_exp_f32 v181, v181\n"
      "v_mfma_f32_16x16x32_bf16 v[156:159], v[168:171], a[252:255], v[156:159]\n"
      "v_exp_f32 v182, v182\n"
      "v_mfma_f32_16x16x32_bf16 v[156:159], v[164:167], a[220:223], v[156:159]\n"
      "buffer_load_dwordx4 v248, s[36:39], s53 offen offset:1024 nt lds\n"
      "v_mfma_f32_16x16x32_bf16 v[164:167], a[160:163], a[32:35], v[196:199]\n"
      "ds_read_b64_tr_b16 a[204:205], v247 offset:264\n"
      "v_cvt_pk_bf16_f32 v178, v180, v181\n"
      "v_mfma_f32_16x16x32_bf16 v[164:167], a[164:167], a[36:39], v[164:167]\n"
      "v_exp_f32 v183, v183\n"
      "v_mfma_f32_16x16x32_bf16 v[164:167], a[168:171], a[40:43], v[164:167]\n"
      "ds_read_b64_tr_b16 a[206:207], v247 offset:4360\n"
      "v_cvt_pk_bf16_f32 v179, v182, v183\n"
      "v_mfma_f32_16x16x32_bf16 v[164:167], a[172:175], a[44:47], v[164:167]\n"
      "ds_read_b64_tr_b16 a[208:209], v247 offset:512\n"
      "v_add_f32 v224, v224, v180\n"
      "v_mfma_f32_16x16x32_bf16 v[160:163], a[188:191], a[44:47], v[196:199]\n"
      "ds_read_b64_tr_b16 a[210:211], v247 offset:4608\n"
      "v_add_f32 v224, v224, v181\n"
      "v_mfma_f32_16x16x32_bf16 v[160:163], a[184:187], a[40:43], v[160:163]\n"
      "ds_read_b64_tr_b16 a[212:213], v247 offset:520\n"
      "v_add_f32 v224, v224, v182\n"
      "v_mfma_f32_16x16x32_bf16 v[160:163], a[180:183], a[36:39], v[160:163]\n"
      "ds_read_b64_tr_b16 a[214:215], v247 offset:4616\n"
      "v_add_f32 v224, v224, v183\n"
      "v_mfma_f32_16x16x32_bf16 v[160:163], a[176:179], a[32:35], v[160:163]\n"
      "s_waitcnt lgkmcnt(6)\n"
      "v_exp_f32 v164, v164\n"
      "v_mfma_f32_16x16x32_bf16 v[168:171], a[128:131], a[32:35], v[196:199]\n"
      "v_exp_f32 v165, v165\n"
      "v_mfma_f32_16x16x32_bf16 v[168:171], a[132:135], a[36:39], v[168:171]\n"
      "v_exp_f32 v166, v166\n"
      "v_mfma_f32_16x16x32_bf16 v[168:171], a[136:139], a[40:43], v[168:171]\n"
      "v_exp_f32 v167, v167\n"
      "v_mfma_f32_16x16x32_bf16 v[168:171], a[140:143], a[44:47], v[168:171]\n"
      "v_exp_f32 v160, v160\n"
      "v_mfma_f32_16x16x32_bf16 v[172:175], a[156:159], a[44:47], v[196:199]\n"
      "buffer_load_dwordx4 v249, s[36:39], s53 offen offset:2048 nt lds\n"
      "v_mfma_f32_16x16x32_bf16 v[172:175], a[152:155], a[40:43], v[172:175]\n"
      "v_exp_f32 v161, v161\n"
      "v_mfma_f32_16x16x32_bf16 v[172:175], a[148:151], a[36:39], v[172:175]\n"
      "v_exp_f32 v162, v162\n"
      "v_mfma_f32_16x16x32_bf16 v[172:175], a[144:147], a[32:35], v[172:175]\n"
      "ds_read_b64_tr_b16 a[216:217], v247 offset:768\n"
      "v_cvt_pk_bf16_f32 v164, v164, v165\n"
      "v_mfma_f32_16x16x32_bf16 v[64:67], v[176:179], a[224:227], v[64:67]\n"
      "v_exp_f32 v168, v168\n"
      "v_mfma_f32_16x16x32_bf16 v[68:71], v[176:179], a[228:231], v[68:71]\n"
      "v_exp_f32 v169, v169\n"
      "v_mfma_f32_16x16x32_bf16 v[72:75], v[176:179], a[232:235], v[72:75]\n"
      "ds_read_b64_tr_b16 a[218:219], v247 offset:4864\n"
      "v_cvt_pk_bf16_f32 v165, v166, v167\n"
      "v_mfma_f32_16x16x32_bf16 v[76:79], v[176:179], a[236:239], v[76:79]\n"
      "v_exp_f32 v170, v170\n"
      "v_mfma_f32_16x16x32_bf16 v[80:83], v[176:179], a[240:243], v[80:83]\n"
      "v_exp_f32 v171, v171\n"
      "v_mfma_f32_16x16x32_bf16 v[84:87], v[176:179], a[244:247], v[84:87]\n"
      "ds_read_b64_tr_b16 a[220:221], v247 offset:776\n"
      "v_cvt_pk_bf16_f32 v166, v160, v161\n"
      "v_mfma_f32_16x16x32_bf16 v[88:91], v[176:179], a[248:251], v[88:91]\n"
      "v_exp_f32 v172, v172\n"
      "v_mfma_f32_16x16x32_bf16 v[92:95], v[176:179], a[252:255], v[92:95]\n"
      "v_exp_f32 v173, v173\n"
      "v_mfma_f32_16x16x32_bf16 v[180:183], a[160:163], a[48:51], v[200:203]\n"
      "buffer_load_dwordx4 v249, s[36:39], s53 offen offset:3072 nt lds\n"
      "v_mfma_f32_16x16x32_bf16 v[180:183], a[164:167], a[52:55], v[180:183]\n"
      "s_add_u32 m0, s55, 4096\n"
      "ds_read_b64_tr_b16 a[222:223], v247 offset:4872\n"
      "v_xor_b32 v247, 0x2000, v247\n"
      "v_mfma_f32_16x16x32_bf16 v[180:183], a[168:171], a[56:59], v[180:183]\n"
      "ds_read_b128 a[160:163], v246 offset:0\n"
      "v_cvt_pk_bf16_f32 v168, v168, v169\n"
      "v_mfma_f32_16x16x32_bf16 v[180:183], a[172:175], a[60:63], v[180:183]\n"
      "v_exp_f32 v174, v174\n"
      "v_mfma_f32_16x16x32_bf16 v[176:179], a[188:191], a[60:63], v[200:203]\n"
      "v_exp_f32 v163, v163\n"
      "v_mfma_f32_16x16x32_bf16 v[176:179], a[184:187], a[56:59], v[176:179]\n"
      "v_exp_f32 v175, v175\n"
      "v_mfma_f32_16x16x32_bf16 v[176:179], a[180:183], a[52:55], v[176:179]\n"
      "v_cvt_pk_bf16_f32 v167, v162, v163\n"
      "v_cvt_pk_bf16_f32 v169, v170, v171\n"
      "v_mfma_f32_16x16x32_bf16 v[176:179], a[176:179], a[48:51], v[176:179]\n"
      "v_cvt_pk_bf16_f32 v170, v172, v173\n"
      "v_cvt_pk_bf16_f32 v171, v174, v175\n"
      "v_mfma_f32_16x16x32_bf16 v[184:187], a[128:131], a[48:51], v[200:203]\n"
      "v_exp_f32 v180, v180\n"
      "v_mfma_f32_16x16x32_bf16 v[184:187], a[132:135], a[52:55], v[184:187]\n"
      "ds_read_b128 a[164:167], v246 offset:256\n"
      "v_add_f32 v220, v220, v180\n"
      "v_mfma_f32_16x16x32_bf16 v[184:187], a[136:139], a[56:59], v[184:187]\n"
      "v_exp_f32 v181, v181\n"
      "v_mfma_f32_16x16x32_bf16 v[184:187], a[140:143], a[60:63], v[184:187]\n"
      "v_exp_f32 v182, v182\n"
      "v_mfma_f32_16x16x32_bf16 v[188:191], a[156:159], a[60:63], v[200:203]\n"
      "v_exp_f32 v183, v183\n"
      "v_mfma_f32_16x16x32_bf16 v[188:191], a[152:155], a[56:59], v[188:191]\n"
      "buffer_load_dwordx4 v248, s[36:39], s54 offen offset:0 nt lds\n"
      "v_mfma_f32_16x16x32_bf16 v[188:191], a[148:151], a[52:55], v[188:191]\n"
      "v_exp_f32 v176, v176\n"
      "v_mfma_f32_16x16x32_bf16 v[188:191], a[144:147], a[48:51], v[188:191]\n"
      "v_exp_f32 v184, v184\n"
      "v_smfmac_f32_16x16x64_bf16 v[216:219], v[240:243], v[164:171], v244\n"
      "ds_read_b128 a[168:171], v246 offset:512\n"
      "v_mfma_f32_16x16x32_bf16 v[0:3], v[164:167], a[224:227], v[0:3]\n"
      "v_exp_f32 v185, v185\n"
      "v_mfma_f32_16x16x32_bf16 v[0:3], v[168:171], a[192:195], v[0:3]\n"
      "v_exp_f32 v186, v186\n"
      "v_mfma_f32_16x16x32_bf16 v[4:7], v[168:171], a[196:199], v[4:7]\n"
      "v_exp_f32 v187, v187\n"
      "v_mfma_f32_16x16x32_bf16 v[4:7], v[164:167], a[228:231], v[4:7]\n"
      "v_exp_f32 v188, v188\n"
      "v_mfma_f32_16x16x32_bf16 v[8:11], v[164:167], a[232:235], v[8:11]\n"
      "v_exp_f32 v177, v177\n"
      "v_mfma_f32_16x16x32_bf16 v[8:11], v[168:171], a[200:203], v[8:11]\n"
      "s_waitcnt lgkmcnt(1)\n"
      "v_exp_f32 v189, v189\n"
      "v_mfma_f32_16x16x32_bf16 v[12:15], v[168:171], a[204:207], v[12:15]\n"
      "ds_read_b128 a[172:175], v246 offset:768\n"
      "v_add_f32 v220, v220, v181\n"
      "v_mfma_f32_16x16x32_bf16 v[12:15], v[164:167], a[236:239], v[12:15]\n"
      "v_exp_f32 v178, v178\n"
      "v_mfma_f32_16x16x32_bf16 v[16:19], v[164:167], a[240:243], v[16:19]\n"
      "buffer_load_dwordx4 v248, s[36:39], s54 offen offset:1024 nt lds\n"
      "v_mfma_f32_16x16x32_bf16 v[16:19], v[168:171], a[208:211], v[16:19]\n"
      "v_exp_f32 v190, v190\n"
      "v_mfma_f32_16x16x32_bf16 v[20:23], v[168:171], a[212:215], v[20:23]\n"
      "v_exp_f32 v179, v179\n"
      "v_mfma_f32_16x16x32_bf16 v[20:23], v[164:167], a[244:247], v[20:23]\n"
      "v_add_f32 v220, v220, v182\n"
      "v_add_f32 v220, v220, v183\n"
      "v_mfma_f32_16x16x32_bf16 v[24:27], v[164:167], a[248:251], v[24:27]\n"
      "v_cvt_pk_bf16_f32 v180, v180, v181\n"
      "v_cvt_pk_bf16_f32 v181, v182, v183\n"
      "v_mfma_f32_16x16x32_bf16 v[24:27], v[168:171], a[216:219], v[24:27]\n"
      "ds_read_b128 a[176:179], v246 offset:4096\n"
      "v_cvt_pk_bf16_f32 v182, v176, v177\n"
      "v_mfma_f32_16x16x32_bf16 v[28:31], v[168:171], a[220:223], v[28:31]\n"
      "v_cvt_pk_bf16_f32 v183, v178, v179\n"
      "v_add_f32 v220, v220, v176\n"
      "v_mfma_f32_16x16x32_bf16 v[28:31], v[164:167], a[252:255], v[28:31]\n"
      "v_exp_f32 v191, v191\n"
      "v_mfma_f32_16x16x32_bf16 v[160:163], a[128:131], a[112:115], v[212:215]\n"
      "ds_read_b128 a[180:183], v246 offset:4352\n"
      "v_add_f32 v220, v220, v177\n"
      "v_mfma_f32_16x16x32_bf16 v[160:163], a[132:135], a[116:119], v[160:163]\n"
      "ds_read_b128 a[184:187], v246 offset:4608\n"
      "v_add_f32 v220, v220, v178\n"
      "v_mfma_f32_16x16x32_bf16 v[160:163], a[136:139], a[120:123], v[160:163]\n"
      "ds_read_b128 a[188:191], v246 offset:4864\n"
      "v_xor_b32 v246, 0x6000, v246\n"
      "v_mfma_f32_16x16x32_bf16 v[160:163], a[140:143], a[124:127], v[160:163]\n"
      "v_add_f32 v220, v220, v179\n"
      "v_add_f32 v220, v220, v184\n"
      "v_mfma_f32_16x16x32_bf16 v[164:167], a[156:159], a[124:127], v[212:215]\n"
      "v_add_f32 v220, v220, v185\n"
      "v_add_f32 v220, v220, v186\n"
      "v_mfma_f32_16x16x32_bf16 v[164:167], a[152:155], a[120:123], v[164:167]\n"
      "buffer_load_dwordx4 v249, s[36:39], s54 offen offset:2048 nt lds\n"
      "v_mfma_f32_16x16x32_bf16 v[164:167], a[148:151], a[116:119], v[164:167]\n"
      "v_add_f32 v220, v220, v187\n"
      "v_cvt_pk_bf16_f32 v184, v184, v185\n"
      "v_mfma_f32_16x16x32_bf16 v[164:167], a[144:147], a[112:115], v[164:167]\n"
      "v_cvt_pk_bf16_f32 v185, v186, v187\n"
      "v_cvt_pk_bf16_f32 v186, v188, v189\n"
      "v_mfma_f32_16x16x32_bf16 v[168:171], a[128:131], a[64:67], v[204:207]\n"
      "v_cvt_pk_bf16_f32 v187, v190, v191\n"
      "v_add_f32 v220, v220, v188\n"
      "v_mfma_f32_16x16x32_bf16 v[168:171], a[132:135], a[68:71], v[168:171]\n"
      "v_exp_f32 v160, v160\n"
      "v_mfma_f32_16x16x32_bf16 v[168:171], a[136:139], a[72:75], v[168:171]\n"
      "v_exp_f32 v161, v161\n"
      "v_mfma_f32_16x16x32_bf16 v[168:171], a[140:143], a[76:79], v[168:171]\n"
      "v_exp_f32 v162, v162\n"
      "v_mfma_f32_16x16x32_bf16 v[172:175], a[156:159], a[76:79], v[204:207]\n"
      "v_exp_f32 v163, v163\n"
      "v_mfma_f32_16x16x32_bf16 v[172:175], a[152:155], a[72:75], v[172:175]\n"
      "v_exp_f32 v164, v164\n"
      "v_mfma_f32_16x16x32_bf16 v[172:175], a[148:151], a[68:71], v[172:175]\n"
      "v_exp_f32 v165, v165\n"
      "v_mfma_f32_16x16x32_bf16 v[172:175], a[144:147], a[64:67], v[172:175]\n"
      "v_exp_f32 v168, v168\n"
      "v_mfma_f32_16x16x32_bf16 v[32:35], v[180:183], a[224:227], v[32:35]\n"
      "ds_read_b64_tr_b16 a[224:225], v247 offset:0\n"
      "v_add_f32 v220, v220, v189\n"
      "v_mfma_f32_16x16x32_bf16 v[32:35], v[184:187], a[192:195], v[32:35]\n"
      "buffer_load_dwordx4 v249, s[36:39], s54 offen offset:3072 nt lds\n"
      "v_mfma_f32_16x16x32_bf16 v[36:39], v[184:187], a[196:199], v[36:39]\n"
      "s_add_u32 m0, s55, 65536\n"
      "ds_read_b64_tr_b16 a[226:227], v247 offset:4096\n"
      "v_add_f32 v220, v220, v190\n"
      "v_mfma_f32_16x16x32_bf16 v[36:39], v[180:183], a[228:231], v[36:39]\n"
      "v_exp_f32 v169, v169\n"
      "v_mfma_f32_16x16x32_bf16 v[40:43], v[180:183], a[232:235], v[40:43]\n"
      "v_add_f32 v220, v220, v191\n"
      "v_cvt_pk_bf16_f32 v160, v160, v161\n"
      "v_mfma_f32_16x16x32_bf16 v[40:43], v[184:187], a[200:203], v[40:43]\n"
      "ds_read_b64_tr_b16 a[228:229], v247 offset:8\n"
      "v_cvt_pk_bf16_f32 v161, v162, v163\n"
      "v_mfma_f32_16x16x32_bf16 v[44:47], v[184:187], a[204:207], v[44:47]\n"
      "v_exp_f32 v166, v166\n"
      "v_mfma_f32_16x16x32_bf16 v[44:47], v[180:183], a[236:239], v[44:47]\n"
      "v_exp_f32 v170, v170\n"
      "v_mfma_f32_16x16x32_bf16 v[48:51], v[180:183], a[240:243], v[48:51]\n"
      "ds_read_b64_tr_b16 a[230:231], v247 offset:4104\n"
      "v_cvt_pk_bf16_f32 v162, v164, v165\n"
      "v_mfma_f32_16x16x32_bf16 v[48:51], v[184:187], a[208:211], v[48:51]\n"
      "v_exp_f32 v167, v167\n"
      "v_mfma_f32_16x16x32_bf16 v[52:55], v[184:187], a[212:215], v[52:55]\n"
      "v_cvt_pk_bf16_f32 v163, v166, v167\n"
      "v_add_f32 v224, v224, v168\n"
      "v_mfma_f32_16x16x32_bf16 v[52:55], v[180:183], a[244:247], v[52:55]\n"
      "v_exp_f32 v171, v171\n"
      "v_mfma_f32_16x16x32_bf16 v[56:59], v[180:183], a[248:251], v[56:59]\n"
      "ds_read_b64_tr_b16 a[232:233], v247 offset:256\n"
      "v_add_f32 v224, v224, v169\n"
      "v_mfma_f32_16x16x32_bf16 v[56:59], v[184:187], a[216:219], v[56:59]\n"
      "v_exp_f32 v172, v172\n"
      "v_mfma_f32_16x16x32_bf16 v[60:63], v[184:187], a[220:223], v[60:63]\n"
      "buffer_load_dwordx4 v248, s[40:43], s53 offen offset:0 nt lds\n"
      "v_mfma_f32_16x16x32_bf16 v[60:63], v[180:183], a[252:255], v[60:63]\n"
      "ds_read_b64_tr_b16 a[234:235], v247 offset:4352\n"
      "v_add_f32 v224, v224, v170\n"
      "v_mfma_f32_16x16x32_bf16 v[180:183], a[128:131], a[80:83], v[208:211]\n"
      "v_exp_f32 v173, v173\n"
      "v_mfma_f32_16x16x32_bf16 v[180:183], a[132:135], a[84:87], v[180:183]\n"
      "v_exp_f32 v174, v174\n"
      "v_mfma_f32_16x16x32_bf16 v[180:183], a[136:139], a[88:91], v[180:183]\n"
      "ds_read_b64_tr_b16 a[236:237], v247 offset:264\n"
      "v_add_f32 v224, v224, v171\n"
      "v_mfma_f32_16x16x32_bf16 v[180:183], a[140:143], a[92:95], v[180:183]\n"
      "v_exp_f32 v175, v175\n"
      "v_mfma_f32_16x16x32_bf16 v[176:179], a[156:159], a[92:95], v[208:211]\n"
      "v_cvt_pk_bf16_f32 v168, v168, v169\n"
      "v_cvt_pk_bf16_f32 v169, v170, v171\n"
      "v_mfma_f32_16x16x32_bf16 v[176:179], a[152:155], a[88:91], v[176:179]\n"
      "ds_read_b64_tr_b16 a[238:239], v247 offset:4360\n"
      "v_cvt_pk_bf16_f32 v170, v172, v173\n"
      "v_mfma_f32_16x16x32_bf16 v[176:179], a[148:151], a[84:87], v[176:179]\n"
      "v_cvt_pk_bf16_f32 v171, v174, v175\n"
      "v_add_f32 v224, v224, v172\n"
      "v_mfma_f32_16x16x32_bf16 v[176:179], a[144:147], a[80:83], v[176:179]\n"
      "v_exp_f32 v180, v180\n"
      "v_mfma_f32_16x16x32_bf16 v[184:187], a[160:163], a[80:83], v[208:211]\n"
      "v_exp_f32 v181, v181\n"
      "v_mfma_f32_16x16x32_bf16 v[184:187], a[164:167], a[84:87], v[184:187]\n"
      "s_waitcnt lgkmcnt(2)\n"
      "ds_read_b64_tr_b16 a[240:241], v247 offset:512\n"
      "v_add_f32 v224, v224, v173\n"
      "v_mfma_f32_16x16x32_bf16 v[184:187], a[168:171], a[88:91], v[184:187]\n"
      "buffer_load_dwordx4 v248, s[40:43], s53 offen offset:1024 nt lds\n"
      "v_mfma_f32_16x16x32_bf16 v[184:187], a[172:175], a[92:95], v[184:187]\n"
      "v_exp_f32 v182, v182\n"
      "v_mfma_f32_16x16x32_bf16 v[188:191], a[188:191], a[92:95], v[208:211]\n"
      "ds_read_b64_tr_b16 a[242:243], v247 offset:4608\n"
      "v_add_f32 v224, v224, v174\n"
      "v_mfma_f32_16x16x32_bf16 v[188:191], a[184:187], a[88:91], v[188:191]\n"
      "v_exp_f32 v183, v183\n"
      "v_mfma_f32_16x16x32_bf16 v[188:191], a[180:183], a[84:87], v[188:191]\n"
      "v_exp_f32 v176, v176\n"
      "v_mfma_f32_16x16x32_bf16 v[188:191], a[176:179], a[80:83], v[188:191]\n"
      "ds_read_b64_tr_b16 a[244:245], v247 offset:520\n"
      "v_add_f32 v224, v224, v175\n"
      "v_mfma_f32_16x16x32_bf16 v[236:239], v[240:243], v[160:163], v[236:239]\n"
      "v_exp_f32 v184, v184\n"
      "v_mfma_f32_16x16x32_bf16 a[0:3], a[192:195], v[160:163], a[0:3]\n"
      "v_exp_f32 v185, v185\n"
      "v_mfma_f32_16x16x32_bf16 a[4:7], a[196:199], v[160:163], a[4:7]\n"
      "ds_read_b64_tr_b16 a[246:247], v247 offset:4616\n"
      "v_add_f32 v228, v228, v180\n"
      "v_mfma_f32_16x16x32_bf16 a[8:11], a[200:203], v[160:163], a[8:11]\n"
      "v_exp_f32 v186, v186\n"
      "v_mfma_f32_16x16x32_bf16 a[12:15], a[204:207], v[160:163], a[12:15]\n"
      "v_exp_f32 v187, v187\n"
      "v_mfma_f32_16x16x32_bf16 a[16:19], a[208:211], v[160:163], a[16:19]\n"
      "v_exp_f32 v188, v188\n"
      "v_mfma_f32_16x16x32_bf16 a[20:23], a[212:215], v[160:163], a[20:23]\n"
      "ds_read_b64_tr_b16 a[248:249], v247 offset:768\n"
      "v_add_f32 v228, v228, v181\n"
      "v_mfma_f32_16x16x32_bf16 a[24:27], a[216:219], v[160:163], a[24:27]\n"
      "buffer_load_dwordx4 v248, s[40:43], s53 offen offset:2048 nt lds\n"
      "v_mfma_f32_16x16x32_bf16 a[28:31], a[220:223], v[160:163], a[28:31]\n"
      "v_exp_f32 v177, v177\n"
      "v_mfma_f32_16x16x32_bf16 v[64:67], v[168:171], a[192:195], v[64:67]\n"
      "ds_read_b64_tr_b16 a[250:251], v247 offset:4864\n"
      "v_add_f32 v228, v228, v182\n"
      "v_mfma_f32_16x16x32_bf16 v[68:71], v[168:171], a[196:199], v[68:71]\n"
      "v_exp_f32 v189, v189\n"
      "v_mfma_f32_16x16x32_bf16 v[72:75], v[168:171], a[200:203], v[72:75]\n"
      "v_exp_f32 v178, v178\n"
      "v_mfma_f32_16x16x32_bf16 v[76:79], v[168:171], a[204:207], v[76:79]\n"
      "ds_read_b64_tr_b16 a[252:253], v247 offset:776\n"
      "v_add_f32 v228, v228, v183\n"
      "v_mfma_f32_16x16x32_bf16 v[80:83], v[168:171], a[208:211], v[80:83]\n"
      "v_exp_f32 v190, v190\n"
      "v_mfma_f32_16x16x32_bf16 v[84:87], v[168:171], a[212:215], v[84:87]\n"
      "v_exp_f32 v179, v179\n"
      "v_mfma_f32_16x16x32_bf16 v[88:91], v[168:171], a[216:219], v[88:91]\n"
      "ds_read_b64_tr_b16 a[254:255], v247 offset:4872\n"
      "v_xor_b32 v247, 0x6000, v247\n"
      "v_mfma_f32_16x16x32_bf16 v[92:95], v[168:171], a[220:223], v[92:95]\n"
      "v_cvt_pk_bf16_f32 v180, v180, v181\n"
      "v_cvt_pk_bf16_f32 v181, v182, v183\n"
      "v_mfma_f32_16x16x32_bf16 v[164:167], a[128:131], a[96:99], v[192:195]\n"
      "v_cvt_pk_bf16_f32 v182, v176, v177\n"
      "v_cvt_pk_bf16_f32 v183, v178, v179\n"
      "v_mfma_f32_16x16x32_bf16 v[164:167], a[132:135], a[100:103], v[164:167]\n"
      "v_exp_f32 v191, v191\n"
      "v_mfma_f32_16x16x32_bf16 v[164:167], a[136:139], a[104:107], v[164:167]\n"
      "buffer_load_dwordx4 v248, s[40:43], s53 offen offset:3072 nt lds\n"
      "v_mfma_f32_16x16x32_bf16 v[164:167], a[140:143], a[108:111], v[164:167]\n"
      "s_add_u32 m0, s55, 69632\n"
      "ds_read_b128 a[128:131], v246 offset:0\n"
      "v_add_f32 v228, v228, v176\n"
      "v_mfma_f32_16x16x32_bf16 v[160:163], a[156:159], a[108:111], v[192:195]\n"
      "s_add_u32 s53, s53, 32768\n"
      "ds_read_b128 a[132:135], v246 offset:256\n"
      "v_add_f32 v228, v228, v177\n"
      "v_mfma_f32_16x16x32_bf16 v[160:163], a[152:155], a[104:107], v[160:163]\n"
      "ds_read_b128 a[136:139], v246 offset:512\n"
      "v_add_f32 v228, v228, v178\n"
      "v_mfma_f32_16x16x32_bf16 v[160:163], a[148:151], a[100:103], v[160:163]\n"
      "ds_read_b128 a[140:143], v246 offset:768\n"
      "v_add_f32 v228, v228, v179\n"
      "v_mfma_f32_16x16x32_bf16 v[160:163], a[144:147], a[96:99], v[160:163]\n"
      "v_exp_f32 v164, v164\n"
      "v_mfma_f32_16x16x32_bf16 v[168:171], a[160:163], a[96:99], v[192:195]\n"
      "v_exp_f32 v165, v165\n"
      "v_mfma_f32_16x16x32_bf16 v[168:171], a[164:167], a[100:103], v[168:171]\n"
      "v_add_f32 v228, v228, v184\n"
      "v_add_f32 v228, v228, v185\n"
      "v_mfma_f32_16x16x32_bf16 v[168:171], a[168:171], a[104:107], v[168:171]\n"
      "v_add_f32 v228, v228, v186\n"
      "v_add_f32 v228, v228, v187\n"
      "v_mfma_f32_16x16x32_bf16 v[168:171], a[172:175], a[108:111], v[168:171]\n"
      "v_cvt_pk_bf16_f32 v184, v184, v185\n"
      "v_cvt_pk_bf16_f32 v185, v186, v187\n"
      "v_mfma_f32_16x16x32_bf16 v[172:175], a[188:191], a[108:111], v[192:195]\n"
      "v_cvt_pk_bf16_f32 v186, v188, v189\n"
      "v_cvt_pk_bf16_f32 v187, v190, v191\n"
      "v_mfma_f32_16x16x32_bf16 v[172:175], a[184:187], a[104:107], v[172:175]\n"
      "v_exp_f32 v166, v166\n"
      "v_mfma_f32_16x16x32_bf16 v[172:175], a[180:183], a[100:103], v[172:175]\n"
      "v_exp_f32 v167, v167\n"
      "v_mfma_f32_16x16x32_bf16 v[172:175], a[176:179], a[96:99], v[172:175]\n"
      "buffer_load_dwordx4 v248, s[40:43], s54 offen offset:0 nt lds\n"
      "v_mfma_f32_16x16x32_bf16 v[96:99], v[180:183], a[192:195], v[96:99]\n"
      ".long 0xBEE500FF\n"
      "PAD2%=:\n"
      "s_setpc_b64 s[94:95]\n"
      "v_exp_f32 v168, v168\n"
      "v_mfma_f32_16x16x32_bf16 v[96:99], v[184:187], a[224:227], v[96:99]\n"
      "v_exp_f32 v169, v169\n"
      "v_mfma_f32_16x16x32_bf16 v[100:103], v[184:187], a[228:231], v[100:103]\n"
      "v_exp_f32 v170, v170\n"
      "v_mfma_f32_16x16x32_bf16 v[100:103], v[180:183], a[196:199], v[100:103]\n"
      "v_exp_f32 v171, v171\n"
      "v_mfma_f32_16x16x32_bf16 v[104:107], v[180:183], a[200:203], v[104:107]\n"
      "v_exp_f32 v160, v160\n"
      "v_mfma_f32_16x16x32_bf16 v[104:107], v[184:187], a[232:235], v[104:107]\n"
      "s_waitcnt lgkmcnt(0)\n"
      "v_exp_f32 v172, v172\n"
      "v_mfma_f32_16x16x32_bf16 v[108:111], v[184:187], a[236:239], v[108:111]\n"
      "v_exp_f32 v161, v161\n"
      "v_mfma_f32_16x16x32_bf16 v[108:111], v[180:183], a[204:207], v[108:111]\n"
      "v_exp_f32 v173, v173\n"
      "v_mfma_f32_16x16x32_bf16 v[112:115], v[180:183], a[208:211], v[112:115]\n"
      "v_exp_f32 v162, v162\n"
      "v_mfma_f32_16x16x32_bf16 v[112:115], v[184:187], a[240:243], v[112:115]\n"
      "v_exp_f32 v174, v174\n"
      "v_mfma_f32_16x16x32_bf16 v[116:119], v[184:187], a[244:247], v[116:119]\n"
      "ds_read_b128 a[144:147], v246 offset:4096\n"
      "v_cvt_pk_bf16_f32 v164, v164, v165\n"
      "v_mfma_f32_16x16x32_bf16 v[116:119], v[180:183], a[212:215], v[116:119]\n"
      "buffer_load_dwordx4 v248, s[40:43], s54 offen offset:1024 nt lds\n"
      "v_mfma_f32_16x16x32_bf16 v[120:123], v[180:183], a[216:219], v[120:123]\n"
      "v_exp_f32 v163, v163\n"
      "v_mfma_f32_16x16x32_bf16 v[120:123], v[184:187], a[248:251], v[120:123]\n"
      "v_exp_f32 v175, v175\n"
      "v_mfma_f32_16x16x32_bf16 v[124:127], v[184:187], a[252:255], v[124:127]\n"
      "v_cvt_pk_bf16_f32 v165, v166, v167\n"
      "v_cvt_pk_bf16_f32 v166, v160, v161\n"
      "v_mfma_f32_16x16x32_bf16 v[124:127], v[180:183], a[220:223], v[124:127]\n"
      "v_cvt_pk_bf16_f32 v167, v162, v163\n"
      "v_cvt_pk_bf16_f32 v168, v168, v169\n"
      "v_mfma_f32_16x16x32_bf16 v[176:179], a[160:163], a[64:67], v[204:207]\n"
      "ds_read_b128 a[148:151], v246 offset:4352\n"
      "v_cvt_pk_bf16_f32 v169, v170, v171\n"
      "v_mfma_f32_16x16x32_bf16 v[176:179], a[164:167], a[68:71], v[176:179]\n"
      "v_cvt_pk_bf16_f32 v170, v172, v173\n"
      "v_cvt_pk_bf16_f32 v171, v174, v175\n"
      "v_mfma_f32_16x16x32_bf16 v[176:179], a[168:171], a[72:75], v[176:179]\n"
      "ds_read_b128 a[152:155], v246 offset:4608\n"
      "v_add_f32 v228, v228, v188\n"
      "v_mfma_f32_16x16x32_bf16 v[176:179], a[172:175], a[76:79], v[176:179]\n"
      "ds_read_b128 a[156:159], v246 offset:4864\n"
      "v_xor_b32 v246, 0x2000, v246\n"
      "v_mfma_f32_16x16x32_bf16 v[180:183], a[188:191], a[76:79], v[204:207]\n"
      "v_add_f32 v228, v228, v189\n"
      "v_add_f32 v228, v228, v190\n"
      "v_mfma_f32_16x16x32_bf16 v[180:183], a[184:187], a[72:75], v[180:183]\n"
      "v_add_f32 v228, v228, v191\n"
      "v_mfma_f32_16x16x32_bf16 v[180:183], a[180:183], a[68:71], v[180:183]\n"
      "v_mfma_f32_16x16x32_bf16 v[180:183], a[176:179], a[64:67], v[180:183]\n"
      "v_exp_f32 v176, v176\n"
      "v_smfmac_f32_16x16x64_bf16 v[232:235], v[240:243], v[164:171], v244\n"
      "v_add_f32 v224, v224, v176\n"
      "v_mfma_f32_16x16x32_bf16 v[128:131], v[164:167], a[192:195], v[128:131]\n"
      "buffer_load_dwordx4 v248, s[40:43], s54 offen offset:2048 nt lds\n"
      "v_mfma_f32_16x16x32_bf16 v[128:131], v[168:171], a[224:227], v[128:131]\n"
      "v_exp_f32 v177, v177\n"
      "v_mfma_f32_16x16x32_bf16 v[132:135], v[168:171], a[228:231], v[132:135]\n"
      "ds_read_b64_tr_b16 a[192:193], v247 offset:0\n"
      "v_add_f32 v224, v224, v177\n"
      "v_mfma_f32_16x16x32_bf16 v[132:135], v[164:167], a[196:199], v[132:135]\n"
      "ds_read_b64_tr_b16 a[194:195], v247 offset:4096\n"
      "v_cvt_pk_bf16_f32 v176, v176, v177\n"
      "v_mfma_f32_16x16x32_bf16 v[136:139], v[164:167], a[200:203], v[136:139]\n"
      "v_exp_f32 v178, v178\n"
      "v_mfma_f32_16x16x32_bf16 v[136:139], v[168:171], a[232:235], v[136:139]\n"
      "ds_read_b64_tr_b16 a[196:197], v247 offset:8\n"
      "v_add_f32 v224, v224, v178\n"
      "v_mfma_f32_16x16x32_bf16 v[140:143], v[168:171], a[236:239], v[140:143]\n"
      "v_exp_f32 v179, v179\n"
      "v_mfma_f32_16x16x32_bf16 v[140:143], v[164:167], a[204:207], v[140:143]\n"
      "ds_read_b64_tr_b16 a[198:199], v247 offset:4104\n"
      "v_add_f32 v224, v224, v179\n"
      "v_mfma_f32_16x16x32_bf16 v[144:147], v[164:167], a[208:211], v[144:147]\n"
      "v_exp_f32 v180, v180\n"
      "v_mfma_f32_16x16x32_bf16 v[144:147], v[168:171], a[240:243], v[144:147]\n"
      "ds_read_b64_tr_b16 a[200:201], v247 offset:256\n"
      "v_cvt_pk_bf16_f32 v177, v178, v179\n"
      "v_mfma_f32_16x16x32_bf16 v[148:151], v[168:171], a[244:247], v[148:151]\n"
      "v_exp_f32 v181, v181\n"
      "v_mfma_f32_16x16x32_bf16 v[148:151], v[164:167], a[212:215], v[148:151]\n"
      "buffer_load_dwordx4 v248, s[40:43], s54 offen offset:3072 nt lds\n"
      "v_mfma_f32_16x16x32_bf16 v[152:155], v[164:167], a[216:219], v[152:155]\n"
      "s_add_u32 s54, s54, 32768\n"
      "ds_read_b64_tr_b16 a[202:203], v247 offset:4352\n"
      "v_cvt_pk_bf16_f32 v178, v180, v181\n"
      "v_mfma_f32_16x16x32_bf16 v[152:155], v[168:171], a[248:251], v[152:155]\n"
      "s_xor_b32 s55, s55, 0x8000\n"
      "v_exp_f32 v182, v182\n"
      "v_mfma_f32_16x16x32_bf16 v[156:159], v[168:171], a[252:255], v[156:159]\n"
      "ds_read_b64_tr_b16 a[204:205], v247 offset:264\n"
      "v_add_f32 v224, v224, v180\n"
      "v_mfma_f32_16x16x32_bf16 v[156:159], v[164:167], a[220:223], v[156:159]\n"
      "v_exp_f32 v183, v183\n"
      "v_mfma_f32_16x16x32_bf16 v[164:167], a[160:163], a[32:35], v[196:199]\n"
      "v_cvt_pk_bf16_f32 v179, v182, v183\n"
      "v_add_f32 v224, v224, v181\n"
      "v_mfma_f32_16x16x32_bf16 v[164:167], a[164:167], a[36:39], v[164:167]\n"
      "ds_read_b64_tr_b16 a[206:207], v247 offset:4360\n"
      "v_add_f32 v224, v224, v182\n"
      "v_mfma_f32_16x16x32_bf16 v[164:167], a[168:171], a[40:43], v[164:167]\n"
      "ds_read_b64_tr_b16 a[208:209], v247 offset:512\n"
      "v_add_f32 v224, v224, v183\n"
      "v_mfma_f32_16x16x32_bf16 v[164:167], a[172:175], a[44:47], v[164:167]\n"
      "ds_read_b64_tr_b16 a[210:211], v247 offset:4608\n"
      "ds_read_b64_tr_b16 a[212:213], v247 offset:520\n"
      "v_mfma_f32_16x16x32_bf16 v[160:163], a[188:191], a[44:47], v[196:199]\n"
      "ds_read_b64_tr_b16 a[214:215], v247 offset:4616\n"
      "ds_read_b64_tr_b16 a[216:217], v247 offset:768\n"
      "v_mfma_f32_16x16x32_bf16 v[160:163], a[184:187], a[40:43], v[160:163]\n"
      "ds_read_b64_tr_b16 a[218:219], v247 offset:4864\n"
      "ds_read_b64_tr_b16 a[220:221], v247 offset:776\n"
      "v_mfma_f32_16x16x32_bf16 v[160:163], a[180:183], a[36:39], v[160:163]\n"
      "ds_read_b64_tr_b16 a[222:223], v247 offset:4872\n"
      "v_xor_b32 v247, 0x2000, v247\n"
      "v_mfma_f32_16x16x32_bf16 v[160:163], a[176:179], a[32:35], v[160:163]\n"
      "v_exp_f32 v164, v164\n"
      "v_mfma_f32_16x16x32_bf16 v[168:171], a[128:131], a[32:35], v[196:199]\n"
      "v_exp_f32 v165, v165\n"
      "v_mfma_f32_16x16x32_bf16 v[168:171], a[132:135], a[36:39], v[168:171]\n"
      "v_exp_f32 v166, v166\n"
      "v_mfma_f32_16x16x32_bf16 v[168:171], a[136:139], a[40:43], v[168:171]\n"
      "v_exp_f32 v167, v167\n"
      "v_mfma_f32_16x16x32_bf16 v[168:171], a[140:143], a[44:47], v[168:171]\n"
      "s_waitcnt lgkmcnt(5)\n"
      "v_exp_f32 v160, v160\n"
      "v_mfma_f32_16x16x32_bf16 v[172:175], a[156:159], a[44:47], v[196:199]\n"
      "v_exp_f32 v161, v161\n"
      "v_mfma_f32_16x16x32_bf16 v[172:175], a[152:155], a[40:43], v[172:175]\n"
      "v_exp_f32 v162, v162\n"
      "v_mfma_f32_16x16x32_bf16 v[172:175], a[148:151], a[36:39], v[172:175]\n"
      "v_exp_f32 v163, v163\n"
      "v_mfma_f32_16x16x32_bf16 v[172:175], a[144:147], a[32:35], v[172:175]\n"
      "v_exp_f32 v168, v168\n"
      "v_mfma_f32_16x16x32_bf16 v[64:67], v[176:179], a[224:227], v[64:67]\n"
      "v_exp_f32 v169, v169\n"
      "v_mfma_f32_16x16x32_bf16 v[68:71], v[176:179], a[228:231], v[68:71]\n"
      "v_exp_f32 v170, v170\n"
      "v_mfma_f32_16x16x32_bf16 v[72:75], v[176:179], a[232:235], v[72:75]\n"
      "v_exp_f32 v171, v171\n"
      "v_mfma_f32_16x16x32_bf16 v[76:79], v[176:179], a[236:239], v[76:79]\n"
      "v_exp_f32 v172, v172\n"
      "v_mfma_f32_16x16x32_bf16 v[80:83], v[176:179], a[240:243], v[80:83]\n"
      "v_exp_f32 v173, v173\n"
      "v_mfma_f32_16x16x32_bf16 v[84:87], v[176:179], a[244:247], v[84:87]\n"
      "v_exp_f32 v174, v174\n"
      "v_mfma_f32_16x16x32_bf16 v[88:91], v[176:179], a[248:251], v[88:91]\n"
      "v_exp_f32 v175, v175\n"
      "v_mfma_f32_16x16x32_bf16 v[92:95], v[176:179], a[252:255], v[92:95]\n"
      "v_add_f32 v216, v216, v164\n"
      "v_add_f32 v216, v216, v165\n"
      "v_mfma_f32_16x16x32_bf16 v[180:183], a[160:163], a[48:51], v[200:203]\n"
      "v_add_f32 v216, v216, v166\n"
      "v_add_f32 v216, v216, v167\n"
      "v_mfma_f32_16x16x32_bf16 v[180:183], a[164:167], a[52:55], v[180:183]\n"
      "ds_read_b128 a[160:163], v246 offset:0\n"
      "v_cvt_pk_bf16_f32 v164, v164, v165\n"
      "v_mfma_f32_16x16x32_bf16 v[180:183], a[168:171], a[56:59], v[180:183]\n"
      "v_cvt_pk_bf16_f32 v165, v166, v167\n"
      "v_cvt_pk_bf16_f32 v166, v160, v161\n"
      "v_mfma_f32_16x16x32_bf16 v[180:183], a[172:175], a[60:63], v[180:183]\n"
      "v_cvt_pk_bf16_f32 v167, v162, v163\n"
      "v_add_f32 v216, v216, v160\n"
      "v_mfma_f32_16x16x32_bf16 v[176:179], a[188:191], a[60:63], v[200:203]\n"
      "ds_read_b128 a[164:167], v246 offset:256\n"
      "v_add_f32 v216, v216, v161\n"
      "v_mfma_f32_16x16x32_bf16 v[176:179], a[184:187], a[56:59], v[176:179]\n"
      "ds_read_b128 a[168:171], v246 offset:512\n"
      "v_add_f32 v216, v216, v162\n"
      "v_mfma_f32_16x16x32_bf16 v[176:179], a[180:183], a[52:55], v[176:179]\n"
      "ds_read_b128 a[172:175], v246 offset:768\n"
      "v_add_f32 v216, v216, v163\n"
      "v_mfma_f32_16x16x32_bf16 v[176:179], a[176:179], a[48:51], v[176:179]\n"
      "v_exp_f32 v180, v180\n"
      "v_mfma_f32_16x16x32_bf16 v[184:187], a[128:131], a[48:51], v[200:203]\n"
      "v_add_f32 v216, v216, v168\n"
      "v_add_f32 v216, v216, v169\n"
      "v_mfma_f32_16x16x32_bf16 v[184:187], a[132:135], a[52:55], v[184:187]\n"
      "v_add_f32 v216, v216, v170\n"
      "v_add_f32 v216, v216, v171\n"
      "v_mfma_f32_16x16x32_bf16 v[184:187], a[136:139], a[56:59], v[184:187]\n"
      "v_cvt_pk_bf16_f32 v168, v168, v169\n"
      "v_cvt_pk_bf16_f32 v169, v170, v171\n"
      "v_mfma_f32_16x16x32_bf16 v[184:187], a[140:143], a[60:63], v[184:187]\n"
      "v_cvt_pk_bf16_f32 v170, v172, v173\n"
      "v_cvt_pk_bf16_f32 v171, v174, v175\n"
      "v_mfma_f32_16x16x32_bf16 v[188:191], a[156:159], a[60:63], v[200:203]\n"
      "v_exp_f32 v181, v181\n"
      "v_mfma_f32_16x16x32_bf16 v[188:191], a[152:155], a[56:59], v[188:191]\n"
      "v_exp_f32 v182, v182\n"
      "v_mfma_f32_16x16x32_bf16 v[188:191], a[148:151], a[52:55], v[188:191]\n"
      "v_exp_f32 v183, v183\n"
      "v_mfma_f32_16x16x32_bf16 v[188:191], a[144:147], a[48:51], v[188:191]\n"
      "v_exp_f32 v184, v184\n"
      "v_mfma_f32_16x16x32_bf16 v[0:3], v[164:167], a[224:227], v[0:3]\n"
      "v_exp_f32 v185, v185\n"
      "v_mfma_f32_16x16x32_bf16 v[0:3], v[168:171], a[192:195], v[0:3]\n"
      "v_exp_f32 v186, v186\n"
      "v_mfma_f32_16x16x32_bf16 v[4:7], v[168:171], a[196:199], v[4:7]\n"
      "v_exp_f32 v187, v187\n"
      "v_mfma_f32_16x16x32_bf16 v[4:7], v[164:167], a[228:231], v[4:7]\n"
      "v_exp_f32 v176, v176\n"
      "v_mfma_f32_16x16x32_bf16 v[8:11], v[164:167], a[232:235], v[8:11]\n"
      "v_exp_f32 v188, v188\n"
      "v_mfma_f32_16x16x32_bf16 v[8:11], v[168:171], a[200:203], v[8:11]\n"
      "v_exp_f32 v177, v177\n"
      "v_mfma_f32_16x16x32_bf16 v[12:15], v[168:171], a[204:207], v[12:15]\n"
      "v_exp_f32 v189, v189\n"
      "v_mfma_f32_16x16x32_bf16 v[12:15], v[164:167], a[236:239], v[12:15]\n"
      "v_exp_f32 v178, v178\n"
      "v_mfma_f32_16x16x32_bf16 v[16:19], v[164:167], a[240:243], v[16:19]\n"
      "v_exp_f32 v190, v190\n"
      "v_mfma_f32_16x16x32_bf16 v[16:19], v[168:171], a[208:211], v[16:19]\n"
      "s_waitcnt lgkmcnt(0)\n"
      "ds_read_b128 a[176:179], v246 offset:4096\n"
      "v_add_f32 v220, v220, v180\n"
      "v_mfma_f32_16x16x32_bf16 v[20:23], v[168:171], a[212:215], v[20:23]\n"
      "v_exp_f32 v179, v179\n"
      "v_mfma_f32_16x16x32_bf16 v[20:23], v[164:167], a[244:247], v[20:23]\n"
      "v_add_f32 v220, v220, v181\n"
      "v_add_f32 v220, v220, v182\n"
      "v_mfma_f32_16x16x32_bf16 v[24:27], v[164:167], a[248:251], v[24:27]\n"
      "v_add_f32 v220, v220, v183\n"
      "v_cvt_pk_bf16_f32 v180, v180, v181\n"
      "v_mfma_f32_16x16x32_bf16 v[24:27], v[168:171], a[216:219], v[24:27]\n"
      "v_cvt_pk_bf16_f32 v181, v182, v183\n"
      "v_cvt_pk_bf16_f32 v182, v176, v177\n"
      "v_mfma_f32_16x16x32_bf16 v[28:31], v[168:171], a[220:223], v[28:31]\n"
      "v_cvt_pk_bf16_f32 v183, v178, v179\n"
      "v_add_f32 v220, v220, v176\n"
      "v_mfma_f32_16x16x32_bf16 v[28:31], v[164:167], a[252:255], v[28:31]\n"
      "ds_read_b128 a[180:183], v246 offset:4352\n"
      "v_add_f32 v220, v220, v177\n"
      "v_mfma_f32_16x16x32_bf16 v[160:163], a[128:131], a[64:67], v[204:207]\n"
      "v_exp_f32 v191, v191\n"
      "v_mfma_f32_16x16x32_bf16 v[160:163], a[132:135], a[68:71], v[160:163]\n"
      "v_add_f32 v220, v220, v178\n"
      "v_add_f32 v220, v220, v179\n"
      "v_mfma_f32_16x16x32_bf16 v[160:163], a[136:139], a[72:75], v[160:163]\n"
      "v_add_f32 v220, v220, v184\n"
      "v_add_f32 v220, v220, v185\n"
      "v_mfma_f32_16x16x32_bf16 v[160:163], a[140:143], a[76:79], v[160:163]\n"
      "v_add_f32 v220, v220, v186\n"
      "v_add_f32 v220, v220, v187\n"
      "v_mfma_f32_16x16x32_bf16 v[164:167], a[156:159], a[76:79], v[204:207]\n"
      "v_cvt_pk_bf16_f32 v184, v184, v185\n"
      "v_cvt_pk_bf16_f32 v185, v186, v187\n"
      "v_mfma_f32_16x16x32_bf16 v[164:167], a[152:155], a[72:75], v[164:167]\n"
      "v_cvt_pk_bf16_f32 v186, v188, v189\n"
      "v_cvt_pk_bf16_f32 v187, v190, v191\n"
      "v_mfma_f32_16x16x32_bf16 v[164:167], a[148:151], a[68:71], v[164:167]\n"
      "ds_read_b128 a[184:187], v246 offset:4608\n"
      "v_add_f32 v220, v220, v188\n"
      "v_mfma_f32_16x16x32_bf16 v[164:167], a[144:147], a[64:67], v[164:167]\n"
      "v_exp_f32 v160, v160\n"
      "v_mfma_f32_16x16x32_bf16 v[32:35], v[180:183], a[224:227], v[32:35]\n"
      "ds_read_b64_tr_b16 a[224:225], v247 offset:0\n"
      "v_add_f32 v220, v220, v189\n"
      "v_mfma_f32_16x16x32_bf16 v[32:35], v[184:187], a[192:195], v[32:35]\n"
      "ds_read_b64_tr_b16 a[226:227], v247 offset:4096\n"
      "v_add_f32 v220, v220, v190\n"
      "v_mfma_f32_16x16x32_bf16 v[36:39], v[184:187], a[196:199], v[36:39]\n"
      "v_exp_f32 v161, v161\n"
      "v_mfma_f32_16x16x32_bf16 v[36:39], v[180:183], a[228:231], v[36:39]\n"
      "ds_read_b128 a[188:191], v246 offset:4864\n"
      "v_xor_b32 v246, 0xe000, v246\n"
      "v_mfma_f32_16x16x32_bf16 v[40:43], v[180:183], a[232:235], v[40:43]\n"
      "ds_read_b64_tr_b16 a[228:229], v247 offset:8\n"
      "v_add_f32 v220, v220, v191\n"
      "v_mfma_f32_16x16x32_bf16 v[40:43], v[184:187], a[200:203], v[40:43]\n"
      "v_exp_f32 v162, v162\n"
      "v_mfma_f32_16x16x32_bf16 v[44:47], v[184:187], a[204:207], v[44:47]\n"
      "v_exp_f32 v163, v163\n"
      "v_mfma_f32_16x16x32_bf16 v[44:47], v[180:183], a[236:239], v[44:47]\n"
      "ds_read_b64_tr_b16 a[230:231], v247 offset:4104\n"
      "v_add_f32 v224, v224, v160\n"
      "v_mfma_f32_16x16x32_bf16 v[48:51], v[180:183], a[240:243], v[48:51]\n"
      "v_exp_f32 v164, v164\n"
      "v_mfma_f32_16x16x32_bf16 v[48:51], v[184:187], a[208:211], v[48:51]\n"
      "ds_read_b64_tr_b16 a[232:233], v247 offset:256\n"
      "v_add_f32 v224, v224, v161\n"
      "v_mfma_f32_16x16x32_bf16 v[52:55], v[184:187], a[212:215], v[52:55]\n"
      "v_exp_f32 v165, v165\n"
      "v_mfma_f32_16x16x32_bf16 v[52:55], v[180:183], a[244:247], v[52:55]\n"
      "v_exp_f32 v166, v166\n"
      "v_mfma_f32_16x16x32_bf16 v[56:59], v[180:183], a[248:251], v[56:59]\n"
      "ds_read_b64_tr_b16 a[234:235], v247 offset:4352\n"
      "v_add_f32 v224, v224, v162\n"
      "v_mfma_f32_16x16x32_bf16 v[56:59], v[184:187], a[216:219], v[56:59]\n"
      "v_exp_f32 v167, v167\n"
      "v_mfma_f32_16x16x32_bf16 v[60:63], v[184:187], a[220:223], v[60:63]\n"
      "v_add_f32 v224, v224, v163\n"
      "v_cvt_pk_bf16_f32 v160, v160, v161\n"
      "v_mfma_f32_16x16x32_bf16 v[60:63], v[180:183], a[252:255], v[60:63]\n"
      "ds_read_b64_tr_b16 a[236:237], v247 offset:264\n"
      "v_cvt_pk_bf16_f32 v161, v162, v163\n"
      "v_mfma_f32_16x16x32_bf16 v[180:183], a[128:131], a[80:83], v[208:211]\n"
      "v_cvt_pk_bf16_f32 v162, v164, v165\n"
      "v_cvt_pk_bf16_f32 v163, v166, v167\n"
      "v_mfma_f32_16x16x32_bf16 v[180:183], a[132:135], a[84:87], v[180:183]\n"
      "v_add_f32 v224, v224, v164\n"
      "v_add_f32 v224, v224, v165\n"
      "v_mfma_f32_16x16x32_bf16 v[180:183], a[136:139], a[88:91], v[180:183]\n"
      "ds_read_b64_tr_b16 a[238:239], v247 offset:4360\n"
      "v_add_f32 v224, v224, v166\n"
      "v_mfma_f32_16x16x32_bf16 v[180:183], a[140:143], a[92:95], v[180:183]\n"
      "v_add_f32 v224, v224, v167\n"
      "v_add_f32 v216, v216, v172\n"
      "v_mfma_f32_16x16x32_bf16 v[176:179], a[156:159], a[92:95], v[208:211]\n"
      "ds_read_b64_tr_b16 a[240:241], v247 offset:512\n"
      "v_add_f32 v216, v216, v173\n"
      "v_mfma_f32_16x16x32_bf16 v[176:179], a[152:155], a[88:91], v[176:179]\n"
      "ds_read_b64_tr_b16 a[242:243], v247 offset:4608\n"
      "v_add_f32 v216, v216, v174\n"
      "v_mfma_f32_16x16x32_bf16 v[176:179], a[148:151], a[84:87], v[176:179]\n"
      "ds_read_b64_tr_b16 a[244:245], v247 offset:520\n"
      "v_add_f32 v216, v216, v175\n"
      "v_mfma_f32_16x16x32_bf16 v[176:179], a[144:147], a[80:83], v[176:179]\n"
      "v_exp_f32 v180, v180\n"
      "v_mfma_f32_16x16x32_bf16 v[184:187], a[160:163], a[80:83], v[208:211]\n"
      "v_exp_f32 v181, v181\n"
      "v_mfma_f32_16x16x32_bf16 v[184:187], a[164:167], a[84:87], v[184:187]\n"
      "v_exp_f32 v182, v182\n"
      "v_mfma_f32_16x16x32_bf16 v[184:187], a[168:171], a[88:91], v[184:187]\n"
      "v_exp_f32 v183, v183\n"
      "v_mfma_f32_16x16x32_bf16 v[184:187], a[172:175], a[92:95], v[184:187]\n"
      "s_waitcnt lgkmcnt(3)\n"
      "v_exp_f32 v176, v176\n"
      "v_mfma_f32_16x16x32_bf16 v[188:191], a[188:191], a[92:95], v[208:211]\n"
      "v_exp_f32 v177, v177\n"
      "v_mfma_f32_16x16x32_bf16 v[188:191], a[184:187], a[88:91], v[188:191]\n"
      "ds_read_b64_tr_b16 a[246:247], v247 offset:4616\n"
      "v_cvt_pk_bf16_f32 v180, v180, v181\n"
      "v_mfma_f32_16x16x32_bf16 v[188:191], a[180:183], a[84:87], v[188:191]\n"
      "v_exp_f32 v178, v178\n"
      "v_mfma_f32_16x16x32_bf16 v[188:191], a[176:179], a[80:83], v[188:191]\n"
      "ds_read_b64_tr_b16 a[248:249], v247 offset:768\n"
      "v_cvt_pk_bf16_f32 v181, v182, v183\n"
      "v_mfma_f32_16x16x32_bf16 v[64:67], v[160:163], a[192:195], v[64:67]\n"
      "v_exp_f32 v184, v184\n"
      "v_mfma_f32_16x16x32_bf16 v[68:71], v[160:163], a[196:199], v[68:71]\n"
      "v_exp_f32 v185, v185\n"
      "v_mfma_f32_16x16x32_bf16 v[72:75], v[160:163], a[200:203], v[72:75]\n"
      "ds_read_b64_tr_b16 a[250:251], v247 offset:4864\n"
      "v_cvt_pk_bf16_f32 v182, v176, v177\n"
      "v_mfma_f32_16x16x32_bf16 v[76:79], v[160:163], a[204:207], v[76:79]\n"
      "v_exp_f32 v186, v186\n"
      "v_mfma_f32_16x16x32_bf16 v[80:83], v[160:163], a[208:211], v[80:83]\n"
      "v_exp_f32 v187, v187\n"
      "v_mfma_f32_16x16x32_bf16 v[84:87], v[160:163], a[212:215], v[84:87]\n"
      "ds_read_b64_tr_b16 a[252:253], v247 offset:776\n"
      "v_cvt_pk_bf16_f32 v184, v184, v185\n"
      "v_mfma_f32_16x16x32_bf16 v[88:91], v[160:163], a[216:219], v[88:91]\n"
      "v_exp_f32 v188, v188\n"
      "v_mfma_f32_16x16x32_bf16 v[92:95], v[160:163], a[220:223], v[92:95]\n"
      "v_exp_f32 v189, v189\n"
      "v_mfma_f32_16x16x32_bf16 v[164:167], a[128:131], a[96:99], v[192:195]\n"
      "ds_read_b64_tr_b16 a[254:255], v247 offset:4872\n"
      "v_xor_b32 v247, 0xe000, v247\n"
      "v_mfma_f32_16x16x32_bf16 v[164:167], a[132:135], a[100:103], v[164:167]\n"
      "v_exp_f32 v190, v190\n"
      "v_mfma_f32_16x16x32_bf16 v[164:167], a[136:139], a[104:107], v[164:167]\n"
      "v_exp_f32 v179, v179\n"
      "v_mfma_f32_16x16x32_bf16 v[164:167], a[140:143], a[108:111], v[164:167]\n"
      "v_exp_f32 v191, v191\n"
      "v_mfma_f32_16x16x32_bf16 v[160:163], a[156:159], a[108:111], v[192:195]\n"
      "v_cvt_pk_bf16_f32 v183, v178, v179\n"
      "v_cvt_pk_bf16_f32 v185, v186, v187\n"
      "v_mfma_f32_16x16x32_bf16 v[160:163], a[152:155], a[104:107], v[160:163]\n"
      "v_cvt_pk_bf16_f32 v186, v188, v189\n"
      "v_cvt_pk_bf16_f32 v187, v190, v191\n"
      "v_mfma_f32_16x16x32_bf16 v[160:163], a[148:151], a[100:103], v[160:163]\n"
      "v_mfma_f32_16x16x32_bf16 v[160:163], a[144:147], a[96:99], v[160:163]\n"
      "v_exp_f32 v164, v164\n"
      "v_mfma_f32_16x16x32_bf16 v[168:171], a[160:163], a[96:99], v[192:195]\n"
      "v_exp_f32 v165, v165\n"
      "v_mfma_f32_16x16x32_bf16 v[168:171], a[164:167], a[100:103], v[168:171]\n"
      "v_exp_f32 v166, v166\n"
      "v_mfma_f32_16x16x32_bf16 v[168:171], a[168:171], a[104:107], v[168:171]\n"
      "v_exp_f32 v167, v167\n"
      "v_mfma_f32_16x16x32_bf16 v[168:171], a[172:175], a[108:111], v[168:171]\n"
      "v_exp_f32 v160, v160\n"
      "v_mfma_f32_16x16x32_bf16 v[172:175], a[188:191], a[108:111], v[192:195]\n"
      "s_cmp_lg_u32 s54, s61\n"
      "v_exp_f32 v161, v161\n"
      "v_mfma_f32_16x16x32_bf16 v[172:175], a[184:187], a[104:107], v[172:175]\n"
      "v_exp_f32 v162, v162\n"
      "v_mfma_f32_16x16x32_bf16 v[172:175], a[180:183], a[100:103], v[172:175]\n"
      "v_exp_f32 v163, v163\n"
      "v_mfma_f32_16x16x32_bf16 v[172:175], a[176:179], a[96:99], v[172:175]\n"
      "s_cbranch_scc1 LOOP%=\n"
      "s_waitcnt vmcnt(0) lgkmcnt(0)\n"
      "ds_add_u32 v254, v255\n"
      "v_smfmac_f32_16x16x64_bf16 v[228:231], v[240:243], v[180:187], v244\n"
      "v_add_f32 v232, v232, v164\n"
      "v_mfma_f32_16x16x32_bf16 v[96:99], v[180:183], a[192:195], v[96:99]\n"
      "v_exp_f32 v168, v168\n"
      "v_mfma_f32_16x16x32_bf16 v[96:99], v[184:187], a[224:227], v[96:99]\n"
      "v_exp_f32 v169, v169\n"
      "v_mfma_f32_16x16x32_bf16 v[100:103], v[184:187], a[228:231], v[100:103]\n"
      "v_exp_f32 v170, v170\n"
      "v_mfma_f32_16x16x32_bf16 v[100:103], v[180:183], a[196:199], v[100:103]\n"
      "v_exp_f32 v171, v171\n"
      "v_mfma_f32_16x16x32_bf16 v[104:107], v[180:183], a[200:203], v[104:107]\n"
      "v_exp_f32 v172, v172\n"
      "v_mfma_f32_16x16x32_bf16 v[104:107], v[184:187], a[232:235], v[104:107]\n"
      "v_exp_f32 v173, v173\n"
      "v_mfma_f32_16x16x32_bf16 v[108:111], v[184:187], a[236:239], v[108:111]\n"
      "ds_read_b32 v253, v254 offset:0\n"
      "v_add_f32 v232, v232, v165\n"
      "v_mfma_f32_16x16x32_bf16 v[108:111], v[180:183], a[204:207], v[108:111]\n"
      "v_exp_f32 v174, v174\n"
      "v_mfma_f32_16x16x32_bf16 v[112:115], v[180:183], a[208:211], v[112:115]\n"
      "v_add_f32 v232, v232, v166\n"
      "v_add_f32 v232, v232, v167\n"
      "v_mfma_f32_16x16x32_bf16 v[112:115], v[184:187], a[240:243], v[112:115]\n"
      "v_cvt_pk_bf16_f32 v164, v164, v165\n"
      "v_cvt_pk_bf16_f32 v165, v166, v167\n"
      "v_mfma_f32_16x16x32_bf16 v[116:119], v[184:187], a[244:247], v[116:119]\n"
      "v_cvt_pk_bf16_f32 v166, v160, v161\n"
      "v_cvt_pk_bf16_f32 v167, v162, v163\n"
      "v_mfma_f32_16x16x32_bf16 v[116:119], v[180:183], a[212:215], v[116:119]\n"
      "v_exp_f32 v175, v175\n"
      "v_mfma_f32_16x16x32_bf16 v[120:123], v[180:183], a[216:219], v[120:123]\n"
      "s_waitcnt lgkmcnt(0)\n"
      "v_cmp_gt_u32 vcc, s100, v253\n"
      "v_add_f32 v232, v232, v160\n"
      "v_mfma_f32_16x16x32_bf16 v[120:123], v[184:187], a[248:251], v[120:123]\n"
      "s_cbranch_vccnz SPINB%=\n"
      "BACKB%=:\n"
      "v_add_f32 v232, v232, v161\n"
      "v_add_f32 v232, v232, v162\n"
      "v_mfma_f32_16x16x32_bf16 v[124:127], v[184:187], a[252:255], v[124:127]\n"
      "s_addk_i32 s100, 4\n"
      "ds_read_b128 a[128:131], v246 offset:0\n"
      "v_add_f32 v232, v232, v163\n"
      "v_mfma_f32_16x16x32_bf16 v[124:127], v[180:183], a[220:223], v[124:127]\n"
      "s_add_u32 s58, s62, 135168\n"
      "ds_read_b128 a[132:135], v246 offset:256\n"
      "v_add_f32 v232, v232, v168\n"
      "v_mfma_f32_16x16x32_bf16 v[176:179], a[160:163], a[64:67], v[204:207]\n"
      "s_lshr_b32 s59, s52, 1\n"
      "v_add_f32 v232, v232, v169\n"
      "v_add_f32 v232, v232, v170\n"
      "v_mfma_f32_16x16x32_bf16 v[176:179], a[164:167], a[68:71], v[176:179]\n"
      "s_lshl_b32 s59, s59, 16\n"
      "ds_read_b128 a[136:139], v246 offset:512\n"
      "v_add_f32 v232, v232, v171\n"
      "v_mfma_f32_16x16x32_bf16 v[176:179], a[168:171], a[72:75], v[176:179]\n"
      "s_and_b32 s60, s52, 1\n"
      "v_cvt_pk_bf16_f32 v168, v168, v169\n"
      "v_cvt_pk_bf16_f32 v169, v170, v171\n"
      "v_mfma_f32_16x16x32_bf16 v[176:179], a[172:175], a[76:79], v[176:179]\n"
      "s_mul_i32 s60, s60, 8448\n"
      "ds_read_b128 a[140:143], v246 offset:768\n"
      "v_cvt_pk_bf16_f32 v170, v172, v173\n"
      "v_mfma_f32_16x16x32_bf16 v[180:183], a[188:191], a[76:79], v[204:207]\n"
      "s_add_u32 s59, s59, s60\n"
      "v_cvt_pk_bf16_f32 v171, v174, v175\n"
      "v_add_f32 v232, v232, v172\n"
      "v_mfma_f32_16x16x32_bf16 v[180:183], a[184:187], a[72:75], v[180:183]\n"
      "s_lshl2_add_u32 s60, s52, 139264\n"
      "v_add_f32 v232, v232, v173\n"
      "v_add_f32 v232, v232, v174\n"
      "v_mfma_f32_16x16x32_bf16 v[180:183], a[180:183], a[68:71], v[180:183]\n"
      "s_mul_i32 s53, s52, 2112\n"
      "ds_read_b128 a[144:147], v246 offset:4096\n"
      "v_add_f32 v232, v232, v175\n"
      "v_mfma_f32_16x16x32_bf16 v[180:183], a[176:179], a[64:67], v[180:183]\n"
      "s_lshl_b32 s54, s52, 6\n"
      "v_exp_f32 v176, v176\n"
      "v_mfma_f32_16x16x32_bf16 v[128:131], v[164:167], a[192:195], v[128:131]\n"
      "s_add_u32 s54, s54, 139264\n"
      "ds_read_b64_tr_b16 a[192:193], v247 offset:0\n"
      "v_add_f32 v224, v224, v176\n"
      "v_mfma_f32_16x16x32_bf16 v[128:131], v[168:171], a[224:227], v[128:131]\n"
      "s_lshl_b32 s55, s52, 10\n"
      "ds_read_b128 a[148:151], v246 offset:4352\n"
      "v_mbcnt_lo_u32_b32 v254, -1, 0\n"
      "v_mfma_f32_16x16x32_bf16 v[132:135], v[168:171], a[228:231], v[132:135]\n"
      "s_add_u32 s69, s55, 81920\n"
      "ds_read_b64_tr_b16 a[194:195], v247 offset:4096\n"
      "v_mbcnt_hi_u32_b32 v254, -1, v254\n"
      "v_mfma_f32_16x16x32_bf16 v[132:135], v[164:167], a[196:199], v[132:135]\n"
      "ds_read_b128 a[152:155], v246 offset:4608\n"
      "v_and_b32 v248, 15, v254\n"
      "v_mfma_f32_16x16x32_bf16 v[136:139], v[164:167], a[200:203], v[136:139]\n"
      "ds_read_b64_tr_b16 a[196:197], v247 offset:8\n"
      "v_lshrrev_b32 v249, 4, v254\n"
      "v_mfma_f32_16x16x32_bf16 v[136:139], v[168:171], a[232:235], v[136:139]\n"
      "v_exp_f32 v177, v177\n"
      "v_mfma_f32_16x16x32_bf16 v[140:143], v[168:171], a[236:239], v[140:143]\n"
      "ds_read_b128 a[156:159], v246 offset:4864\n"
      "v_xor_b32 v246, 0x2000, v246\n"
      "v_mfma_f32_16x16x32_bf16 v[140:143], v[164:167], a[204:207], v[140:143]\n"
      "ds_read_b64_tr_b16 a[198:199], v247 offset:4104\n"
      "v_add_f32 v224, v224, v177\n"
      "v_mfma_f32_16x16x32_bf16 v[144:147], v[164:167], a[208:211], v[144:147]\n"
      "v_exp_f32 v178, v178\n"
      "v_mfma_f32_16x16x32_bf16 v[144:147], v[168:171], a[240:243], v[144:147]\n"
      "ds_read_b64_tr_b16 a[200:201], v247 offset:256\n"
      "v_add_f32 v224, v224, v178\n"
      "v_mfma_f32_16x16x32_bf16 v[148:151], v[168:171], a[244:247], v[148:151]\n"
      "v_exp_f32 v179, v179\n"
      "v_mfma_f32_16x16x32_bf16 v[148:151], v[164:167], a[212:215], v[148:151]\n"
      "v_exp_f32 v180, v180\n"
      "v_mfma_f32_16x16x32_bf16 v[152:155], v[164:167], a[216:219], v[152:155]\n"
      "ds_read_b64_tr_b16 a[202:203], v247 offset:4352\n"
      "v_add_f32 v224, v224, v179\n"
      "v_mfma_f32_16x16x32_bf16 v[152:155], v[168:171], a[248:251], v[152:155]\n"
      "v_exp_f32 v181, v181\n"
      "v_mfma_f32_16x16x32_bf16 v[156:159], v[168:171], a[252:255], v[156:159]\n"
      "v_exp_f32 v182, v182\n"
      "v_mfma_f32_16x16x32_bf16 v[156:159], v[164:167], a[220:223], v[156:159]\n"
      "ds_read_b64_tr_b16 a[204:205], v247 offset:264\n"
      "v_cvt_pk_bf16_f32 v176, v176, v177\n"
      "v_mfma_f32_16x16x32_bf16 v[164:167], a[160:163], a[32:35], v[196:199]\n"
      "v_exp_f32 v183, v183\n"
      "v_mfma_f32_16x16x32_bf16 v[164:167], a[164:167], a[36:39], v[164:167]\n"
      "v_cvt_pk_bf16_f32 v177, v178, v179\n"
      "v_cvt_pk_bf16_f32 v178, v180, v181\n"
      "v_mfma_f32_16x16x32_bf16 v[164:167], a[168:171], a[40:43], v[164:167]\n"
      "ds_read_b64_tr_b16 a[206:207], v247 offset:4360\n"
      "v_cvt_pk_bf16_f32 v179, v182, v183\n"
      "v_mfma_f32_16x16x32_bf16 v[164:167], a[172:175], a[44:47], v[164:167]\n"
      "v_mul_u32_u24 v252, 528, v248\n"
      "v_lshl_add_u32 v252, v249, 5, v252\n"
      "v_mfma_f32_16x16x32_bf16 v[160:163], a[188:191], a[44:47], v[196:199]\n"
      "ds_read_b64_tr_b16 a[208:209], v247 offset:512\n"
      "v_add_u32 v252, s59, v252\n"
      "v_mfma_f32_16x16x32_bf16 v[160:163], a[184:187], a[40:43], v[160:163]\n"
      "v_lshlrev_b32 v253, 4, v248\n"
      "v_add_u32 v253, s60, v253\n"
      "v_mfma_f32_16x16x32_bf16 v[160:163], a[180:183], a[36:39], v[160:163]\n"
      "v_lshlrev_b32 v250, 10, v249\n"
      "v_lshl_add_u32 v251, v248, 4, v250\n"
      "v_mfma_f32_16x16x32_bf16 v[160:163], a[176:179], a[32:35], v[160:163]\n"
      "s_waitcnt lgkmcnt(2)\n"
      "ds_read_b64_tr_b16 a[210:211], v247 offset:4608\n"
      "v_add_u32 v251, s58, v251\n"
      "v_mfma_f32_16x16x32_bf16 v[168:171], a[128:131], a[32:35], v[196:199]\n"
      "v_and_b32 v245, 3, v248\n"
      "v_lshl_add_u32 v250, v245, 6, v250\n"
      "v_mfma_f32_16x16x32_bf16 v[168:171], a[132:135], a[36:39], v[168:171]\n"
      "v_and_b32 v245, 12, v248\n"
      "v_lshl_add_u32 v250, v245, 2, v250\n"
      "v_mfma_f32_16x16x32_bf16 v[168:171], a[136:139], a[40:43], v[168:171]\n"
      "ds_read_b64_tr_b16 a[212:213], v247 offset:520\n"
      "v_add_u32 v245, s58, v250\n"
      "v_mfma_f32_16x16x32_bf16 v[168:171], a[140:143], a[44:47], v[168:171]\n"
      "v_mov_b32 v255, 1.0\n"
      "v_add_f32 v224, v224, v180\n"
      "v_mfma_f32_16x16x32_bf16 v[172:175], a[156:159], a[44:47], v[196:199]\n"
      "v_exp_f32 v164, v164\n"
      "v_mfma_f32_16x16x32_bf16 v[172:175], a[152:155], a[40:43], v[172:175]\n"
      "ds_read_b64_tr_b16 a[214:215], v247 offset:4616\n"
      "v_add_f32 v224, v224, v181\n"
      "v_mfma_f32_16x16x32_bf16 v[172:175], a[148:151], a[36:39], v[172:175]\n"
      "v_exp_f32 v165, v165\n"
      "v_mfma_f32_16x16x32_bf16 v[172:175], a[144:147], a[32:35], v[172:175]\n"
      "ds_read_b64_tr_b16 a[216:217], v247 offset:768\n"
      "v_add_f32 v224, v224, v182\n"
      "v_mfma_f32_16x16x32_bf16 v[64:67], v[176:179], a[224:227], v[64:67]\n"
      "v_add_f32 v224, v224, v183\n"
      "v_cvt_pk_bf16_f32 v164, v164, v165\n"
      "v_mfma_f32_16x16x32_bf16 v[68:71], v[176:179], a[228:231], v[68:71]\n"
      "v_exp_f32 v168, v168\n"
      "v_mfma_f32_16x16x32_bf16 v[72:75], v[176:179], a[232:235], v[72:75]\n"
      "v_exp_f32 v169, v169\n"
      "v_mfma_f32_16x16x32_bf16 v[76:79], v[176:179], a[236:239], v[76:79]\n"
      "ds_read_b64_tr_b16 a[218:219], v247 offset:4864\n"
      "v_cvt_pk_bf16_f32 v168, v168, v169\n"
      "v_mfma_f32_16x16x32_bf16 v[80:83], v[176:179], a[240:243], v[80:83]\n"
      "v_exp_f32 v166, v166\n"
      "v_mfma_f32_16x16x32_bf16 v[84:87], v[176:179], a[244:247], v[84:87]\n"
      "v_exp_f32 v170, v170\n"
      "v_mfma_f32_16x16x32_bf16 v[88:91], v[176:179], a[248:251], v[88:91]\n"
      "v_exp_f32 v167, v167\n"
      "v_mfma_f32_16x16x32_bf16 v[92:95], v[176:179], a[252:255], v[92:95]\n"
      "ds_read_b64_tr_b16 a[220:221], v247 offset:776\n"
      "v_cvt_pk_bf16_f32 v165, v166, v167\n"
      "v_mfma_f32_16x16x32_bf16 v[180:183], a[160:163], a[48:51], v[200:203]\n"
      "v_exp_f32 v171, v171\n"
      "v_mfma_f32_16x16x32_bf16 v[180:183], a[164:167], a[52:55], v[180:183]\n"
      "ds_read_b64_tr_b16 a[222:223], v247 offset:4872\n"
      "v_xor_b32 v247, 0x2000, v247\n"
      "v_mfma_f32_16x16x32_bf16 v[180:183], a[168:171], a[56:59], v[180:183]\n"
      "ds_read_b128 a[160:163], v246 offset:0\n"
      "v_cvt_pk_bf16_f32 v169, v170, v171\n"
      "v_mfma_f32_16x16x32_bf16 v[180:183], a[172:175], a[60:63], v[180:183]\n"
      "v_exp_f32 v160, v160\n"
      "v_mfma_f32_16x16x32_bf16 v[176:179], a[188:191], a[60:63], v[200:203]\n"
      "v_exp_f32 v172, v172\n"
      "v_mfma_f32_16x16x32_bf16 v[176:179], a[184:187], a[56:59], v[176:179]\n"
      "v_exp_f32 v161, v161\n"
      "v_mfma_f32_16x16x32_bf16 v[176:179], a[180:183], a[52:55], v[176:179]\n"
      "v_exp_f32 v173, v173\n"
      "v_mfma_f32_16x16x32_bf16 v[176:179], a[176:179], a[48:51], v[176:179]\n"
      "v_exp_f32 v162, v162\n"
      "v_mfma_f32_16x16x32_bf16 v[184:187], a[128:131], a[48:51], v[200:203]\n"
      "v_exp_f32 v174, v174\n"
      "v_mfma_f32_16x16x32_bf16 v[184:187], a[132:135], a[52:55], v[184:187]\n"
      "ds_read_b128 a[164:167], v246 offset:256\n"
      "v_cvt_pk_bf16_f32 v166, v160, v161\n"
      "v_mfma_f32_16x16x32_bf16 v[184:187], a[136:139], a[56:59], v[184:187]\n"
      "v_exp_f32 v163, v163\n"
      "v_mfma_f32_16x16x32_bf16 v[184:187], a[140:143], a[60:63], v[184:187]\n"
      "v_exp_f32 v175, v175\n"
      "v_mfma_f32_16x16x32_bf16 v[188:191], a[156:159], a[60:63], v[200:203]\n"
      "v_cvt_pk_bf16_f32 v167, v162, v163\n"
      "v_cvt_pk_bf16_f32 v170, v172, v173\n"
      "v_mfma_f32_16x16x32_bf16 v[188:191], a[152:155], a[56:59], v[188:191]\n"
      "v_exp_f32 v180, v180\n"
      "v_mfma_f32_16x16x32_bf16 v[188:191], a[148:151], a[52:55], v[188:191]\n"
      "ds_read_b128 a[168:171], v246 offset:512\n"
      "v_cvt_pk_bf16_f32 v171, v174, v175\n"
      "v_mfma_f32_16x16x32_bf16 v[188:191], a[144:147], a[48:51], v[188:191]\n"
      "v_exp_f32 v184, v184\n"
      "v_smfmac_f32_16x16x64_bf16 v[216:219], v[240:243], v[164:171], v244\n"
      "v_mfma_f32_16x16x32_bf16 v[0:3], v[164:167], a[224:227], v[0:3]\n"
      ".long 0xBEE500FF\n"
      "PAD3%=:\n"
      "s_setpc_b64 s[94:95]\n"
      "v_exp_f32 v181, v181\n"
      "v_mfma_f32_16x16x32_bf16 v[0:3], v[168:171], a[192:195], v[0:3]\n"
      "v_exp_f32 v185, v185\n"
      "v_mfma_f32_16x16x32_bf16 v[4:7], v[168:171], a[196:199], v[4:7]\n"
      "v_exp_f32 v182, v182\n"
      "v_mfma_f32_16x16x32_bf16 v[4:7], v[164:167], a[228:231], v[4:7]\n"
      "ds_read_b128 a[172:175], v246 offset:768\n"
      "v_cvt_pk_bf16_f32 v180, v180, v181\n"
      "v_mfma_f32_16x16x32_bf16 v[8:11], v[164:167], a[232:235], v[8:11]\n"
      "v_exp_f32 v186, v186\n"
      "v_mfma_f32_16x16x32_bf16 v[8:11], v[168:171], a[200:203], v[8:11]\n"
      "s_waitcnt lgkmcnt(1)\n"
      "v_exp_f32 v183, v183\n"
      "v_mfma_f32_16x16x32_bf16 v[12:15], v[168:171], a[204:207], v[12:15]\n"
      "v_exp_f32 v187, v187\n"
      "v_mfma_f32_16x16x32_bf16 v[12:15], v[164:167], a[236:239], v[12:15]\n"
      "v_exp_f32 v176, v176\n"
      "v_mfma_f32_16x16x32_bf16 v[16:19], v[164:167], a[240:243], v[16:19]\n"
      "v_exp_f32 v188, v188\n"
      "v_mfma_f32_16x16x32_bf16 v[16:19], v[168:171], a[208:211], v[16:19]\n"
      "ds_read_b128 a[176:179], v246 offset:4096\n"
      "v_cvt_pk_bf16_f32 v181, v182, v183\n"
      "v_mfma_f32_16x16x32_bf16 v[20:23], v[168:171], a[212:215], v[20:23]\n"
      "v_exp_f32 v177, v177\n"
      "v_mfma_f32_16x16x32_bf16 v[20:23], v[164:167], a[244:247], v[20:23]\n"
      "v_exp_f32 v189, v189\n"
      "v_mfma_f32_16x16x32_bf16 v[24:27], v[164:167], a[248:251], v[24:27]\n"
      "v_exp_f32 v178, v178\n"
      "v_mfma_f32_16x16x32_bf16 v[24:27], v[168:171], a[216:219], v[24:27]\n"
      "v_exp_f32 v190, v190\n"
      "v_mfma_f32_16x16x32_bf16 v[28:31], v[168:171], a[220:223], v[28:31]\n"
      "v_exp_f32 v179, v179\n"
      "v_mfma_f32_16x16x32_bf16 v[28:31], v[164:167], a[252:255], v[28:31]\n"
      "ds_read_b128 a[180:183], v246 offset:4352\n"
      "v_cvt_pk_bf16_f32 v182, v176, v177\n"
      "v_mfma_f32_16x16x32_bf16 v[160:163], a[128:131], a[112:115], v[212:215]\n"
      "v_exp_f32 v191, v191\n"
      "v_mfma_f32_16x16x32_bf16 v[160:163], a[132:135], a[116:119], v[160:163]\n"
      "v_cvt_pk_bf16_f32 v183, v178, v179\n"
      "v_cvt_pk_bf16_f32 v184, v184, v185\n"
      "v_mfma_f32_16x16x32_bf16 v[160:163], a[136:139], a[120:123], v[160:163]\n"
      "ds_read_b128 a[184:187], v246 offset:4608\n"
      "v_cvt_pk_bf16_f32 v185, v186, v187\n"
      "v_mfma_f32_16x16x32_bf16 v[160:163], a[140:143], a[124:127], v[160:163]\n"
      "v_cvt_pk_bf16_f32 v186, v188, v189\n"
      "v_cvt_pk_bf16_f32 v187, v190, v191\n"
      "v_mfma_f32_16x16x32_bf16 v[164:167], a[156:159], a[124:127], v[212:215]\n"
      "ds_read_b128 a[188:191], v246 offset:4864\n"
      "v_xor_b32 v246, 0x6000, v246\n"
      "v_mfma_f32_16x16x32_bf16 v[164:167], a[152:155], a[120:123], v[164:167]\n"
      "v_mfma_f32_16x16x32_bf16 v[164:167], a[148:151], a[116:119], v[164:167]\n"
      "v_mul_u32_u24 v212, 528, v249\n"
      "v_lshl_add_u32 v212, v248, 5, v212\n"
      "v_mfma_f32_16x16x32_bf16 v[164:167], a[144:147], a[112:115], v[164:167]\n"
      "v_exp_f32 v160, v160\n"
      "v_smfmac_f32_16x16x64_bf16 v[220:223], v[240:243], v[180:187], v244\n"
      "v_add_u32 v212, s53, v212\n"
      "v_mfma_f32_16x16x32_bf16 v[32:35], v[180:183], a[224:227], v[32:35]\n"
      "ds_read_b64_tr_b16 a[224:225], v247 offset:0\n"
      "v_add_u32 v213, 0x10000, v212\n"
      "v_mfma_f32_16x16x32_bf16 v[32:35], v[184:187], a[192:195], v[32:35]\n"
      "ds_read_b64_tr_b16 a[226:227], v247 offset:4096\n"
      "v_lshlrev_b32 v214, 4, v249\n"
      "v_mfma_f32_16x16x32_bf16 v[36:39], v[184:187], a[196:199], v[36:39]\n"
      "v_exp_f32 v161, v161\n"
      "v_mfma_f32_16x16x32_bf16 v[36:39], v[180:183], a[228:231], v[36:39]\n"
      "v_exp_f32 v162, v162\n"
      "v_mfma_f32_16x16x32_bf16 v[40:43], v[180:183], a[232:235], v[40:43]\n"
      "ds_read_b64_tr_b16 a[228:229], v247 offset:8\n"
      "v_cvt_pk_bf16_f32 v160, v160, v161\n"
      "v_mfma_f32_16x16x32_bf16 v[40:43], v[184:187], a[200:203], v[40:43]\n"
      "v_exp_f32 v163, v163\n"
      "v_mfma_f32_16x16x32_bf16 v[44:47], v[184:187], a[204:207], v[44:47]\n"
      "v_exp_f32 v164, v164\n"
      "v_mfma_f32_16x16x32_bf16 v[44:47], v[180:183], a[236:239], v[44:47]\n"
      "ds_read_b64_tr_b16 a[230:231], v247 offset:4104\n"
      "v_cvt_pk_bf16_f32 v161, v162, v163\n"
      "v_mfma_f32_16x16x32_bf16 v[48:51], v[180:183], a[240:243], v[48:51]\n"
      "v_exp_f32 v165, v165\n"
      "v_mfma_f32_16x16x32_bf16 v[48:51], v[184:187], a[208:211], v[48:51]\n"
      "v_exp_f32 v166, v166\n"
      "v_mfma_f32_16x16x32_bf16 v[52:55], v[184:187], a[212:215], v[52:55]\n"
      "ds_read_b64_tr_b16 a[232:233], v247 offset:256\n"
      "v_cvt_pk_bf16_f32 v162, v164, v165\n"
      "v_mfma_f32_16x16x32_bf16 v[52:55], v[180:183], a[244:247], v[52:55]\n"
      "v_exp_f32 v167, v167\n"
      "v_mfma_f32_16x16x32_bf16 v[56:59], v[180:183], a[248:251], v[56:59]\n"
      "ds_read_b64_tr_b16 a[234:235], v247 offset:4352\n"
      "v_cvt_pk_bf16_f32 v163, v166, v167\n"
      "v_mfma_f32_16x16x32_bf16 v[56:59], v[184:187], a[216:219], v[56:59]\n"
      "v_add_u32 v214, s54, v214\n"
      "v_lshlrev_b32 v215, 4, v254\n"
      "v_mfma_f32_16x16x32_bf16 v[60:63], v[184:187], a[220:223], v[60:63]\n"
      "ds_read_b64_tr_b16 a[236:237], v247 offset:264\n"
      "ds_read_b64_tr_b16 a[238:239], v247 offset:4360\n"
      "v_mfma_f32_16x16x32_bf16 v[60:63], v[180:183], a[252:255], v[60:63]\n"
      "ds_read_b64_tr_b16 a[240:241], v247 offset:512\n"
      "ds_read_b64_tr_b16 a[242:243], v247 offset:4608\n"
      "v_mfma_f32_16x16x32_bf16 v[180:183], a[128:131], a[64:67], v[204:207]\n"
      "ds_read_b128 a[128:131], v246 offset:0\n"
      "ds_read_b64_tr_b16 a[244:245], v247 offset:520\n"
      "v_mfma_f32_16x16x32_bf16 v[180:183], a[132:135], a[68:71], v[180:183]\n"
      "ds_read_b128 a[132:135], v246 offset:256\n"
      "ds_read_b64_tr_b16 a[246:247], v247 offset:4616\n"
      "v_mfma_f32_16x16x32_bf16 v[180:183], a[136:139], a[72:75], v[180:183]\n"
      "ds_read_b128 a[136:139], v246 offset:512\n"
      "ds_read_b64_tr_b16 a[248:249], v247 offset:768\n"
      "v_mfma_f32_16x16x32_bf16 v[180:183], a[140:143], a[76:79], v[180:183]\n"
      "ds_read_b128 a[140:143], v246 offset:768\n"
      "ds_read_b64_tr_b16 a[250:251], v247 offset:4864\n"
      "v_mfma_f32_16x16x32_bf16 v[176:179], a[156:159], a[76:79], v[204:207]\n"
      "ds_read_b64_tr_b16 a[252:253], v247 offset:776\n"
      "ds_read_b64_tr_b16 a[254:255], v247 offset:4872\n"
      "v_mfma_f32_16x16x32_bf16 v[176:179], a[152:155], a[72:75], v[176:179]\n"
      "ds_read_b128 a[152:155], v246 offset:4608\n"
      "v_xor_b32 v247, 0x6000, v247\n"
      "v_mfma_f32_16x16x32_bf16 v[176:179], a[148:151], a[68:71], v[176:179]\n"
      "ds_read_b128 a[148:151], v246 offset:4352\n"
      "ds_read_b128 a[156:159], v246 offset:4864\n"
      "v_mfma_f32_16x16x32_bf16 v[176:179], a[144:147], a[64:67], v[176:179]\n"
      "v_exp_f32 v180, v180\n"
      "v_mfma_f32_16x16x32_bf16 v[184:187], a[160:163], a[64:67], v[204:207]\n"
      "v_exp_f32 v181, v181\n"
      "v_mfma_f32_16x16x32_bf16 v[184:187], a[164:167], a[68:71], v[184:187]\n"
      "v_exp_f32 v182, v182\n"
      "v_mfma_f32_16x16x32_bf16 v[184:187], a[168:171], a[72:75], v[184:187]\n"
      "s_waitcnt lgkmcnt(7)\n"
      "v_exp_f32 v183, v183\n"
      "v_mfma_f32_16x16x32_bf16 v[184:187], a[172:175], a[76:79], v[184:187]\n"
      "v_exp_f32 v176, v176\n"
      "v_mfma_f32_16x16x32_bf16 v[188:191], a[188:191], a[76:79], v[204:207]\n"
      "v_exp_f32 v177, v177\n"
      "v_mfma_f32_16x16x32_bf16 v[188:191], a[184:187], a[72:75], v[188:191]\n"
      "v_exp_f32 v178, v178\n"
      "v_mfma_f32_16x16x32_bf16 v[188:191], a[180:183], a[68:71], v[188:191]\n"
      "v_exp_f32 v179, v179\n"
      "v_mfma_f32_16x16x32_bf16 v[188:191], a[176:179], a[64:67], v[188:191]\n"
      "v_exp_f32 v184, v184\n"
      "v_mfma_f32_16x16x32_bf16 v[236:239], v[240:243], v[160:163], v[236:239]\n"
      "v_exp_f32 v185, v185\n"
      "v_mfma_f32_16x16x32_bf16 a[0:3], a[192:195], v[160:163], a[0:3]\n"
      "v_exp_f32 v186, v186\n"
      "v_mfma_f32_16x16x32_bf16 a[4:7], a[196:199], v[160:163], a[4:7]\n"
      "v_exp_f32 v187, v187\n"
      "v_mfma_f32_16x16x32_bf16 a[8:11], a[200:203], v[160:163], a[8:11]\n"
      "v_exp_f32 v188, v188\n"
      "v_mfma_f32_16x16x32_bf16 a[12:15], a[204:207], v[160:163], a[12:15]\n"
      "v_exp_f32 v189, v189\n"
      "v_mfma_f32_16x16x32_bf16 a[16:19], a[208:211], v[160:163], a[16:19]\n"
      "v_exp_f32 v190, v190\n"
      "v_mfma_f32_16x16x32_bf16 a[20:23], a[212:215], v[160:163], a[20:23]\n"
      "ds_read_b128 a[144:147], v246 offset:4096\n"
      "v_xor_b32 v246, 0x2000, v246\n"
      "v_mfma_f32_16x16x32_bf16 a[24:27], a[216:219], v[160:163], a[24:27]\n"
      "v_exp_f32 v191, v191\n"
      "v_mfma_f32_16x16x32_bf16 a[28:31], a[220:223], v[160:163], a[28:31]\n"
      "v_cvt_pk_bf16_f32 v180, v180, v181\n"
      "v_cvt_pk_bf16_f32 v181, v182, v183\n"
      "v_mfma_f32_16x16x32_bf16 v[160:163], a[160:163], a[32:35], v[196:199]\n"
      "ds_write_b128 v252, a[0:3] offset:0\n"
      "v_mfma_f32_16x16x32_bf16 v[160:163], a[164:167], a[36:39], v[160:163]\n"
      "ds_read_b64_tr_b16 a[0:1], v247 offset:0\n"
      "v_cvt_pk_bf16_f32 v182, v176, v177\n"
      "v_mfma_f32_16x16x32_bf16 v[160:163], a[168:171], a[40:43], v[160:163]\n"
      "ds_read_b64_tr_b16 a[2:3], v247 offset:4096\n"
      "v_cvt_pk_bf16_f32 v183, v178, v179\n"
      "v_mfma_f32_16x16x32_bf16 v[160:163], a[172:175], a[44:47], v[160:163]\n"
      "ds_write_b128 v252, a[4:7] offset:16\n"
      "v_mfma_f32_16x16x32_bf16 v[164:167], a[188:191], a[44:47], v[196:199]\n"
      "v_cvt_pk_bf16_f32 v184, v184, v185\n"
      "v_cvt_pk_bf16_f32 v185, v186, v187\n"
      "v_mfma_f32_16x16x32_bf16 v[164:167], a[184:187], a[40:43], v[164:167]\n"
      "ds_read_b64_tr_b16 a[4:5], v247 offset:8\n"
      "v_cvt_pk_bf16_f32 v186, v188, v189\n"
      "v_mfma_f32_16x16x32_bf16 v[164:167], a[180:183], a[36:39], v[164:167]\n"
      "ds_write_b128 v252, a[8:11] offset:128\n"
      "v_mfma_f32_16x16x32_bf16 v[164:167], a[176:179], a[32:35], v[164:167]\n"
      "v_cvt_pk_bf16_f32 v187, v190, v191\n"
      "v_min_u32 v236, 0x7f800000, v236\n"
      "v_mfma_f32_16x16x32_bf16 v[168:171], a[160:163], a[48:51], v[200:203]\n"
      "v_exp_f32 v160, v160\n"
      "v_mfma_f32_16x16x32_bf16 v[168:171], a[164:167], a[52:55], v[168:171]\n"
      "v_exp_f32 v161, v161\n"
      "v_mfma_f32_16x16x32_bf16 v[168:171], a[168:171], a[56:59], v[168:171]\n"
      "ds_read_b128 a[160:163], v246 offset:0\n"
      "v_cvt_pk_bf16_f32 v160, v160, v161\n"
      "v_mfma_f32_16x16x32_bf16 v[168:171], a[172:175], a[60:63], v[168:171]\n"
      "v_exp_f32 v162, v162\n"
      "v_mfma_f32_16x16x32_bf16 v[172:175], a[188:191], a[60:63], v[200:203]\n"
      "v_exp_f32 v163, v163\n"
      "v_mfma_f32_16x16x32_bf16 v[172:175], a[184:187], a[56:59], v[172:175]\n"
      "ds_read_b128 a[164:167], v246 offset:256\n"
      "v_cvt_pk_bf16_f32 v161, v162, v163\n"
      "v_mfma_f32_16x16x32_bf16 v[172:175], a[180:183], a[52:55], v[172:175]\n"
      "ds_write_b128 v252, a[12:15] offset:144\n"
      "v_mfma_f32_16x16x32_bf16 v[172:175], a[176:179], a[48:51], v[172:175]\n"
      "v_exp_f32 v164, v164\n"
      "v_smfmac_f32_16x16x64_bf16 v[224:227], v[240:243], v[180:187], v244\n"
      "ds_read_b64_tr_b16 a[6:7], v247 offset:4104\n"
      "v_mfma_f32_16x16x32_bf16 v[64:67], v[180:183], a[192:195], v[64:67]\n"
      "v_exp_f32 v168, v168\n"
      "v_mfma_f32_16x16x32_bf16 v[64:67], v[184:187], a[224:227], v[64:67]\n"
      "v_exp_f32 v165, v165\n"
      "v_mfma_f32_16x16x32_bf16 v[68:71], v[184:187], a[228:231], v[68:71]\n"
      "ds_read_b128 a[168:171], v246 offset:512\n"
      "v_cvt_pk_bf16_f32 v162, v164, v165\n"
      "v_mfma_f32_16x16x32_bf16 v[68:71], v[180:183], a[196:199], v[68:71]\n"
      "ds_write_b128 v252, a[16:19] offset:256\n"
      "v_mfma_f32_16x16x32_bf16 v[72:75], v[180:183], a[200:203], v[72:75]\n"
      "v_exp_f32 v169, v169\n"
      "v_mfma_f32_16x16x32_bf16 v[72:75], v[184:187], a[232:235], v[72:75]\n"
      "ds_read_b128 a[172:175], v246 offset:768\n"
      "v_cvt_pk_bf16_f32 v168, v168, v169\n"
      "v_mfma_f32_16x16x32_bf16 v[76:79], v[184:187], a[236:239], v[76:79]\n"
      "v_exp_f32 v166, v166\n"
      "v_mfma_f32_16x16x32_bf16 v[76:79], v[180:183], a[204:207], v[76:79]\n"
      "v_exp_f32 v170, v170\n"
      "v_mfma_f32_16x16x32_bf16 v[80:83], v[180:183], a[208:211], v[80:83]\n"
      "v_exp_f32 v167, v167\n"
      "v_mfma_f32_16x16x32_bf16 v[80:83], v[184:187], a[240:243], v[80:83]\n"
      "ds_read_b64_tr_b16 a[8:9], v247 offset:256\n"
      "v_cvt_pk_bf16_f32 v163, v166, v167\n"
      "v_mfma_f32_16x16x32_bf16 v[84:87], v[184:187], a[244:247], v[84:87]\n"
      "ds_write_b128 v252, a[20:23] offset:272\n"
      "v_mfma_f32_16x16x32_bf16 v[84:87], v[180:183], a[212:215], v[84:87]\n"
      "v_exp_f32 v171, v171\n"
      "v_mfma_f32_16x16x32_bf16 v[88:91], v[180:183], a[216:219], v[88:91]\n"
      "s_waitcnt lgkmcnt(3)\n"
      "ds_read_b128 a[176:179], v246 offset:4096\n"
      "v_cvt_pk_bf16_f32 v169, v170, v171\n"
      "v_mfma_f32_16x16x32_bf16 v[88:91], v[184:187], a[248:251], v[88:91]\n"
      "ds_write_b128 v252, a[24:27] offset:384\n"
      "v_mfma_f32_16x16x32_bf16 v[92:95], v[184:187], a[252:255], v[92:95]\n"
      "v_exp_f32 v172, v172\n"
      "v_mfma_f32_16x16x32_bf16 v[92:95], v[180:183], a[220:223], v[92:95]\n"
      "v_exp_f32 v173, v173\n"
      "v_mfma_f32_16x16x32_bf16 v[180:183], a[128:131], a[32:35], v[196:199]\n"
      "ds_read_b128 a[180:183], v246 offset:4352\n"
      "v_cvt_pk_bf16_f32 v170, v172, v173\n"
      "v_mfma_f32_16x16x32_bf16 v[180:183], a[132:135], a[36:39], v[180:183]\n"
      "ds_read_b128 a[188:191], v246 offset:4864\n"
      "v_exp_f32 v174, v174\n"
      "v_mfma_f32_16x16x32_bf16 v[180:183], a[136:139], a[40:43], v[180:183]\n"
      "ds_read_b128 a[184:187], v246 offset:4608\n"
      "v_exp_f32 v175, v175\n"
      "v_mfma_f32_16x16x32_bf16 v[180:183], a[140:143], a[44:47], v[180:183]\n"
      "ds_read_b64_tr_b16 a[10:11], v247 offset:4352\n"
      "v_xor_b32 v246, 0x6000, v246\n"
      "v_mfma_f32_16x16x32_bf16 v[176:179], a[156:159], a[44:47], v[196:199]\n"
      "ds_write_b128 v252, a[28:31] offset:400\n"
      "v_mfma_f32_16x16x32_bf16 v[176:179], a[152:155], a[40:43], v[176:179]\n"
      "ds_read_b64_tr_b16 a[12:13], v247 offset:264\n"
      "v_cvt_pk_bf16_f32 v171, v174, v175\n"
      "v_mfma_f32_16x16x32_bf16 v[176:179], a[148:151], a[36:39], v[176:179]\n"
      "ds_write_b32 v253, v236 offset:256\n"
      "v_mfma_f32_16x16x32_bf16 v[176:179], a[144:147], a[32:35], v[176:179]\n"
      "v_exp_f32 v180, v180\n"
      "v_mfma_f32_16x16x32_bf16 v[184:187], a[160:163], a[32:35], v[196:199]\n"
      "v_exp_f32 v181, v181\n"
      "v_mfma_f32_16x16x32_bf16 v[184:187], a[164:167], a[36:39], v[184:187]\n"
      "ds_read_b64_tr_b16 a[14:15], v247 offset:4360\n"
      "v_cvt_pk_bf16_f32 v180, v180, v181\n"
      "v_mfma_f32_16x16x32_bf16 v[184:187], a[168:171], a[40:43], v[184:187]\n"
      "s_waitcnt lgkmcnt(5)\n"
      "v_exp_f32 v182, v182\n"
      "v_mfma_f32_16x16x32_bf16 v[184:187], a[172:175], a[44:47], v[184:187]\n"
      "v_exp_f32 v183, v183\n"
      "v_mfma_f32_16x16x32_bf16 v[188:191], a[188:191], a[44:47], v[196:199]\n"
      "ds_read_b64_tr_b16 a[16:17], v247 offset:512\n"
      "v_cvt_pk_bf16_f32 v181, v182, v183\n"
      "v_mfma_f32_16x16x32_bf16 v[188:191], a[184:187], a[40:43], v[188:191]\n"
      "v_exp_f32 v176, v176\n"
      "v_mfma_f32_16x16x32_bf16 v[188:191], a[180:183], a[36:39], v[188:191]\n"
      "v_exp_f32 v177, v177\n"
      "v_mfma_f32_16x16x32_bf16 v[188:191], a[176:179], a[32:35], v[188:191]\n"
      "ds_read_b64_tr_b16 a[18:19], v247 offset:4608\n"
      "v_cvt_pk_bf16_f32 v182, v176, v177\n"
      "v_smfmac_f32_16x16x32_bf16 v[216:219], v[240:241], v[160:163], v244\n"
      "v_exp_f32 v184, v184\n"
      "v_mfma_f32_16x16x32_bf16 v[0:3], v[160:163], a[224:227], v[0:3]\n"
      "v_exp_f32 v185, v185\n"
      "v_mfma_f32_16x16x32_bf16 v[4:7], v[160:163], a[228:231], v[4:7]\n"
      "ds_read_b64_tr_b16 a[20:21], v247 offset:520\n"
      "v_cvt_pk_bf16_f32 v184, v184, v185\n"
      "v_mfma_f32_16x16x32_bf16 v[8:11], v[160:163], a[232:235], v[8:11]\n"
      "v_exp_f32 v186, v186\n"
      "v_mfma_f32_16x16x32_bf16 v[12:15], v[160:163], a[236:239], v[12:15]\n"
      "v_exp_f32 v187, v187\n"
      "v_mfma_f32_16x16x32_bf16 v[16:19], v[160:163], a[240:243], v[16:19]\n"
      "ds_read_b64_tr_b16 a[22:23], v247 offset:4616\n"
      "v_cvt_pk_bf16_f32 v185, v186, v187\n"
      "v_mfma_f32_16x16x32_bf16 v[20:23], v[160:163], a[244:247], v[20:23]\n"
      "v_exp_f32 v188, v188\n"
      "v_mfma_f32_16x16x32_bf16 v[24:27], v[160:163], a[248:251], v[24:27]\n"
      "v_exp_f32 v189, v189\n"
      "v_mfma_f32_16x16x32_bf16 v[28:31], v[160:163], a[252:255], v[28:31]\n"
      "ds_read_b128 v[236:239], v214 offset:256\n"
      "v_cvt_pk_bf16_f32 v186, v188, v189\n"
      "v_smfmac_f32_16x16x32_bf16 v[220:223], v[240:241], v[168:171], v244\n"
      "v_exp_f32 v178, v178\n"
      "v_mfma_f32_16x16x32_bf16 v[32:35], v[168:171], a[224:227], v[32:35]\n"
      "v_exp_f32 v190, v190\n"
      "v_mfma_f32_16x16x32_bf16 v[36:39], v[168:171], a[228:231], v[36:39]\n"
      "v_exp_f32 v179, v179\n"
      "v_mfma_f32_16x16x32_bf16 v[40:43], v[168:171], a[232:235], v[40:43]\n"
      "ds_read_b64_tr_b16 a[24:25], v247 offset:768\n"
      "v_cvt_pk_bf16_f32 v183, v178, v179\n"
      "v_mfma_f32_16x16x32_bf16 v[44:47], v[168:171], a[236:239], v[44:47]\n"
      "v_exp_f32 v191, v191\n"
      "v_mfma_f32_16x16x32_bf16 v[48:51], v[168:171], a[240:243], v[48:51]\n"
      "ds_read_b64_tr_b16 a[26:27], v247 offset:4864\n"
      "v_cvt_pk_bf16_f32 v187, v190, v191\n"
      "v_mfma_f32_16x16x32_bf16 v[52:55], v[168:171], a[244:247], v[52:55]\n"
      "ds_read_b64_tr_b16 a[28:29], v247 offset:776\n"
      "ds_read_b64_tr_b16 a[30:31], v247 offset:4872\n"
      "v_mfma_f32_16x16x32_bf16 v[56:59], v[168:171], a[248:251], v[56:59]\n"
      "v_xor_b32 v247, 0x2000, v247\n"
      "v_mfma_f32_16x16x32_bf16 v[60:63], v[168:171], a[252:255], v[60:63]\n"
      "ds_read_b64_tr_b16 a[224:225], v247 offset:0\n"
      "ds_read_b64_tr_b16 a[226:227], v247 offset:4096\n"
      "v_mfma_f32_16x16x32_bf16 v[164:167], a[128:131], a[48:51], v[200:203]\n"
      "s_waitcnt lgkmcnt(6)\n"
      "ds_read_b64_tr_b16 a[228:229], v247 offset:8\n"
      "v_max3_u32 v253, v236, v237, v238\n"
      "v_mfma_f32_16x16x32_bf16 v[164:167], a[132:135], a[52:55], v[164:167]\n"
      "ds_read_b64_tr_b16 a[230:231], v247 offset:4104\n"
      "v_max_u32 v253, v253, v239\n"
      "v_mfma_f32_16x16x32_bf16 v[164:167], a[136:139], a[56:59], v[164:167]\n"
      "ds_read_b64_tr_b16 a[232:233], v247 offset:256\n"
      "v_cmp_eq_u32 vcc, -1, v253\n"
      "v_mfma_f32_16x16x32_bf16 v[164:167], a[140:143], a[60:63], v[164:167]\n"
      "s_cbranch_vccnz HSPN%=\n"
      "HBCK%=:\n"
      "ds_read_b64_tr_b16 a[234:235], v247 offset:4352\n"
      "ds_read_b64_tr_b16 a[236:237], v247 offset:264\n"
      "v_mfma_f32_16x16x32_bf16 v[160:163], a[156:159], a[60:63], v[200:203]\n"
      "ds_read_b64_tr_b16 a[238:239], v247 offset:4360\n"
      "v_add_f32 v236, v236, v237\n"
      "v_mfma_f32_16x16x32_bf16 v[160:163], a[152:155], a[56:59], v[160:163]\n"
      "ds_read_b64_tr_b16 a[240:241], v247 offset:512\n"
      "v_add_f32 v238, v238, v239\n"
      "v_mfma_f32_16x16x32_bf16 v[160:163], a[148:151], a[52:55], v[160:163]\n"
      "ds_read_b64_tr_b16 a[242:243], v247 offset:4608\n"
      "v_add_f32 v236, v236, v238\n"
      "v_mfma_f32_16x16x32_bf16 v[160:163], a[144:147], a[48:51], v[160:163]\n"
      "ds_read_b64_tr_b16 a[244:245], v247 offset:520\n"
      "v_mul_f32 v236, 2.0, v236\n"
      "v_mfma_f32_16x16x32_bf16 v[168:171], a[160:163], a[48:51], v[200:203]\n"
      "v_exp_f32 v164, v164\n"
      "v_mfma_f32_16x16x32_bf16 v[168:171], a[164:167], a[52:55], v[168:171]\n"
      "v_exp_f32 v165, v165\n"
      "v_mfma_f32_16x16x32_bf16 v[168:171], a[168:171], a[56:59], v[168:171]\n"
      "ds_read_b64_tr_b16 a[246:247], v247 offset:4616\n"
      "v_cvt_pk_bf16_f32 v164, v164, v165\n"
      "v_mfma_f32_16x16x32_bf16 v[168:171], a[172:175], a[60:63], v[168:171]\n"
      "v_exp_f32 v166, v166\n"
      "v_mfma_f32_16x16x32_bf16 v[172:175], a[188:191], a[60:63], v[200:203]\n"
      "v_exp_f32 v167, v167\n"
      "v_mfma_f32_16x16x32_bf16 v[172:175], a[184:187], a[56:59], v[172:175]\n"
      "ds_read_b64_tr_b16 a[248:249], v247 offset:768\n"
      "v_cvt_pk_bf16_f32 v165, v166, v167\n"
      "v_mfma_f32_16x16x32_bf16 v[172:175], a[180:183], a[52:55], v[172:175]\n"
      "v_exp_f32 v160, v160\n"
      "v_mfma_f32_16x16x32_bf16 v[172:175], a[176:179], a[48:51], v[172:175]\n"
      "v_exp_f32 v168, v168\n"
      "v_smfmac_f32_16x16x64_bf16 v[216:219], v[240:243], v[180:187], v244\n"
      "ds_read_b64_tr_b16 a[250:251], v247 offset:4864\n"
      "v_mfma_f32_16x16x32_bf16 v[0:3], v[180:183], a[0:3], v[0:3]\n"
      "s_waitcnt lgkmcnt(3)\n"
      "v_exp_f32 v169, v169\n"
      "v_mfma_f32_16x16x32_bf16 v[0:3], v[184:187], a[224:227], v[0:3]\n"
      "ds_read_b64_tr_b16 a[252:253], v247 offset:776\n"
      "v_cvt_pk_bf16_f32 v168, v168, v169\n"
      "v_mfma_f32_16x16x32_bf16 v[4:7], v[184:187], a[228:231], v[4:7]\n"
      "v_exp_f32 v170, v170\n"
      "v_mfma_f32_16x16x32_bf16 v[4:7], v[180:183], a[4:7], v[4:7]\n"
      "ds_read_b64_tr_b16 a[254:255], v247 offset:4872\n"
      "v_xor_b32 v247, 0x4000, v247\n"
      "v_cmp_nge_f32 vcc, 0x5E800000, v216\n"
      "v_mfma_f32_16x16x32_bf16 v[8:11], v[180:183], a[8:11], v[8:11]\n"
      "s_or_b64 s[56:57], s[56:57], vcc\n"
      "v_exp_f32 v171, v171\n"
      "v_mfma_f32_16x16x4_f32 v[196:199], v216, v255, 0\n"
      "ds_read_b64_tr_b16 a[32:33], v247 offset:0\n"
      "v_cvt_pk_bf16_f32 v169, v170, v171\n"
      "v_exp_f32 v172, v172\n"
      "v_mfma_f32_16x16x32_bf16 v[8:11], v[184:187], a[232:235], v[8:11]\n"
      "v_exp_f32 v161, v161\n"
      "v_mfma_f32_16x16x32_bf16 v[12:15], v[184:187], a[236:239], v[12:15]\n"
      "ds_read_b64_tr_b16 a[34:35], v247 offset:4096\n"
      "v_cvt_pk_bf16_f32 v166, v160, v161\n"
      "v_mfma_f32_16x16x32_bf16 v[12:15], v[180:183], a[12:15], v[12:15]\n"
      "v_rcp_f32 v196, v196\n"
      "v_mfma_f32_16x16x32_bf16 v[16:19], v[180:183], a[16:19], v[16:19]\n"
      "v_rcp_f32 v197, v197\n"
      "v_mfma_f32_16x16x32_bf16 v[16:19], v[184:187], a[240:243], v[16:19]\n"
      "s_waitcnt lgkmcnt(3)\n"
      "v_rcp_f32 v198, v198\n"
      "v_mfma_f32_16x16x32_bf16 v[20:23], v[184:187], a[244:247], v[20:23]\n"
      "v_rcp_f32 v199, v199\n"
      "v_mfma_f32_16x16x32_bf16 v[20:23], v[180:183], a[20:23], v[20:23]\n"
      "v_exp_f32 v173, v173\n"
      "v_mfma_f32_16x16x32_bf16 v[24:27], v[180:183], a[24:27], v[24:27]\n"
      "ds_read_b64_tr_b16 a[36:37], v247 offset:8\n"
      "v_cvt_pk_bf16_f32 v170, v172, v173\n"
      "v_mfma_f32_16x16x32_bf16 v[24:27], v[184:187], a[248:251], v[24:27]\n"
      "s_waitcnt lgkmcnt(2)\n"
      "v_exp_f32 v162, v162\n"
      "v_mfma_f32_16x16x32_bf16 v[28:31], v[184:187], a[252:255], v[28:31]\n"
      "v_exp_f32 v174, v174\n"
      "v_mfma_f32_16x16x32_bf16 v[28:31], v[180:183], a[28:31], v[28:31]\n"
      "v_exp_f32 v163, v163\n"
      "v_mfma_f32_16x16x32_bf16 v[180:183], a[128:131], a[64:67], v[204:207]\n"
      "ds_read_b64_tr_b16 a[38:39], v247 offset:4104\n"
      "v_cvt_pk_bf16_f32 v167, v162, v163\n"
      "v_mfma_f32_16x16x32_bf16 v[180:183], a[132:135], a[68:71], v[180:183]\n"
      "v_exp_f32 v175, v175\n"
      "v_mfma_f32_16x16x32_bf16 v[180:183], a[136:139], a[72:75], v[180:183]\n"
      "ds_read_b64_tr_b16 a[40:41], v247 offset:256\n"
      "v_cvt_pk_bf16_f32 v171, v174, v175\n"
      "v_mfma_f32_16x16x32_bf16 v[180:183], a[140:143], a[76:79], v[180:183]\n"
      "v_pk_mul_f32 v[216:217], v[196:197], s[28:29] op_sel:[0,1] op_sel_hi:[1,1]\n"
      "v_pk_mul_f32 v[218:219], v[198:199], s[28:29] op_sel:[0,1] op_sel_hi:[1,1]\n"
      "v_pk_fma_f32 v[0:1], v[0:1], v[196:197], v[216:217]\n"
      "v_pk_fma_f32 v[2:3], v[2:3], v[198:199], v[218:219]\n"
      "v_pk_fma_f32 v[4:5], v[4:5], v[196:197], v[216:217]\n"
      "v_pk_fma_f32 v[6:7], v[6:7], v[198:199], v[218:219]\n"
      "v_pk_fma_f32 v[8:9], v[8:9], v[196:197], v[216:217]\n"
      "v_pk_fma_f32 v[10:11], v[10:11], v[198:199], v[218:219]\n"
      "v_pk_fma_f32 v[12:13], v[12:13], v[196:197], v[216:217]\n"
      "v_pk_fma_f32 v[14:15], v[14:15], v[198:199], v[218:219]\n"
      "v_pk_fma_f32 v[16:17], v[16:17], v[196:197], v[216:217]\n"
      "v_pk_fma_f32 v[18:19], v[18:19], v[198:199], v[218:219]\n"
      "v_pk_fma_f32 v[20:21], v[20:21], v[196:197], v[216:217]\n"
      "v_pk_fma_f32 v[22:23], v[22:23], v[198:199], v[218:219]\n"
      "v_pk_fma_f32 v[24:25], v[24:25], v[196:197], v[216:217]\n"
      "v_pk_fma_f32 v[26:27], v[26:27], v[198:199], v[218:219]\n"
      "v_pk_fma_f32 v[28:29], v[28:29], v[196:197], v[216:217]\n"
      "v_pk_fma_f32 v[30:31], v[30:31], v[198:199], v[218:219]\n"
      "v_mfma_f32_16x16x32_bf16 v[176:179], a[156:159], a[76:79], v[204:207]\n"
      "v_cvt_pk_bf16_f32 v216, v0, v8\n"
      "v_cvt_pk_bf16_f32 v217, v16, v24\n"
      "v_mfma_f32_16x16x32_bf16 v[176:179], a[152:155], a[72:75], v[176:179]\n"
      "v_cvt_pk_bf16_f32 v218, v4, v12\n"
      "v_cvt_pk_bf16_f32 v219, v20, v28\n"
      "v_mfma_f32_16x16x32_bf16 v[176:179], a[148:151], a[68:71], v[176:179]\n"
      "ds_write_b128 v251, v[216:219] offset:0\n"
      "v_mfma_f32_16x16x32_bf16 v[176:179], a[144:147], a[64:67], v[176:179]\n"
      "v_cvt_pk_bf16_f32 v0, v1, v9\n"
      "v_cvt_pk_bf16_f32 v1, v17, v25\n"
      "v_mfma_f32_16x16x32_bf16 v[184:187], a[160:163], a[64:67], v[204:207]\n"
      "v_cvt_pk_bf16_f32 v8, v5, v13\n"
      "v_cvt_pk_bf16_f32 v9, v21, v29\n"
      "v_mfma_f32_16x16x32_bf16 v[184:187], a[164:167], a[68:71], v[184:187]\n"
      "ds_write2_b64 v251, v[0:1], v[8:9] offset0:32 offset1:33\n"
      "v_mfma_f32_16x16x32_bf16 v[184:187], a[168:171], a[72:75], v[184:187]\n"
      "v_cvt_pk_bf16_f32 v16, v2, v10\n"
      "v_cvt_pk_bf16_f32 v17, v18, v26\n"
      "v_mfma_f32_16x16x32_bf16 v[184:187], a[172:175], a[76:79], v[184:187]\n"
      "v_cvt_pk_bf16_f32 v24, v6, v14\n"
      "v_cvt_pk_bf16_f32 v25, v22, v30\n"
      "v_mfma_f32_16x16x32_bf16 v[188:191], a[188:191], a[76:79], v[204:207]\n"
      "ds_write2_b64 v251, v[16:17], v[24:25] offset0:64 offset1:65\n"
      "v_cvt_pk_bf16_f32 v28, v3, v11\n"
      "v_mfma_f32_16x16x32_bf16 v[188:191], a[184:187], a[72:75], v[188:191]\n"
      "v_cvt_pk_bf16_f32 v29, v19, v27\n"
      "v_cvt_pk_bf16_f32 v30, v7, v15\n"
      "v_mfma_f32_16x16x32_bf16 v[188:191], a[180:183], a[68:71], v[188:191]\n"
      "ds_read_b64_tr_b16 v[216:217], v245 offset:0\n"
      "v_cvt_pk_bf16_f32 v31, v23, v31\n"
      "v_mfma_f32_16x16x32_bf16 v[188:191], a[176:179], a[64:67], v[188:191]\n"
      "ds_write_b128 v251, v[28:31] offset:768\n"
      "v_smfmac_f32_16x16x64_bf16 v[220:223], v[240:243], v[164:171], v244\n"
      "ds_read_b64_tr_b16 v[218:219], v245 offset:8\n"
      "ds_read_b64_tr_b16 v[4:5], v245 offset:256\n"
      "v_mfma_f32_16x16x32_bf16 v[32:35], v[164:167], a[0:3], v[32:35]\n"
      "ds_read_b64_tr_b16 v[6:7], v245 offset:264\n"
      "ds_read_b64_tr_b16 v[12:13], v245 offset:512\n"
      "v_mfma_f32_16x16x32_bf16 v[32:35], v[168:171], a[224:227], v[32:35]\n"
      "ds_read_b64_tr_b16 v[14:15], v245 offset:520\n"
      "ds_read_b64_tr_b16 v[28:29], v245 offset:768\n"
      "v_mfma_f32_16x16x32_bf16 v[36:39], v[168:171], a[228:231], v[36:39]\n"
      "ds_read_b64_tr_b16 v[30:31], v245 offset:776\n"
      "ds_read_b64_tr_b16 a[42:43], v247 offset:4352\n"
      "v_mfma_f32_16x16x32_bf16 v[36:39], v[164:167], a[4:7], v[36:39]\n"
      "ds_read_b64_tr_b16 a[44:45], v247 offset:264\n"
      "v_cmp_nge_f32 vcc, 0x5E800000, v220\n"
      "v_mfma_f32_16x16x32_bf16 v[40:43], v[164:167], a[8:11], v[40:43]\n"
      "s_or_b64 s[56:57], s[56:57], vcc\n"
      "v_exp_f32 v180, v180\n"
      "v_mfma_f32_16x16x4_f32 v[200:203], v220, v255, 0\n"
      "v_exp_f32 v184, v184\n"
      "v_exp_f32 v181, v181\n"
      "v_mfma_f32_16x16x32_bf16 v[40:43], v[168:171], a[232:235], v[40:43]\n"
      "v_exp_f32 v185, v185\n"
      "v_mfma_f32_16x16x32_bf16 v[44:47], v[168:171], a[236:239], v[44:47]\n"
      "v_exp_f32 v182, v182\n"
      "v_mfma_f32_16x16x32_bf16 v[44:47], v[164:167], a[12:15], v[44:47]\n"
      "s_waitcnt lgkmcnt(7)\n"
      "buffer_store_dwordx4 v[216:219], v250, s[48:51], s64 offen offset:0 sc0 sc1 nt\n"
      "v_rcp_f32 v200, v200\n"
      "v_cvt_pk_bf16_f32 v180, v180, v181\n"
      "v_mfma_f32_16x16x32_bf16 v[48:51], v[164:167], a[16:19], v[48:51]\n"
      "v_rcp_f32 v201, v201\n"
      "v_mfma_f32_16x16x32_bf16 v[48:51], v[168:171], a[240:243], v[48:51]\n"
      "v_rcp_f32 v202, v202\n"
      "v_mfma_f32_16x16x32_bf16 v[52:55], v[168:171], a[244:247], v[52:55]\n"
      "v_rcp_f32 v203, v203\n"
      "v_mfma_f32_16x16x32_bf16 v[52:55], v[164:167], a[20:23], v[52:55]\n"
      "ds_read_b64_tr_b16 a[46:47], v247 offset:4360\n"
      "v_cvt_pk_bf16_f32 v184, v184, v185\n"
      "v_mfma_f32_16x16x32_bf16 v[56:59], v[164:167], a[24:27], v[56:59]\n"
      "v_exp_f32 v186, v186\n"
      "v_mfma_f32_16x16x32_bf16 v[56:59], v[168:171], a[248:251], v[56:59]\n"
      "v_exp_f32 v183, v183\n"
      "v_mfma_f32_16x16x32_bf16 v[60:63], v[168:171], a[252:255], v[60:63]\n"
      "v_exp_f32 v187, v187\n"
      "v_mfma_f32_16x16x32_bf16 v[60:63], v[164:167], a[28:31], v[60:63]\n"
      "s_waitcnt lgkmcnt(1)\n"
      "buffer_store_dwordx4 v[4:7], v250, s[48:51], s64 offen offset:256 sc0 sc1 nt\n"
      "v_exp_f32 v176, v176\n"
      "v_cvt_pk_bf16_f32 v181, v182, v183\n"
      "v_mfma_f32_16x16x32_bf16 v[164:167], a[128:131], a[80:83], v[208:211]\n"
      "ds_read_b128 a[128:131], v246 offset:0\n"
      "v_cvt_pk_bf16_f32 v185, v186, v187\n"
      "v_mfma_f32_16x16x32_bf16 v[164:167], a[132:135], a[84:87], v[164:167]\n"
      "v_exp_f32 v188, v188\n"
      "v_mfma_f32_16x16x32_bf16 v[164:167], a[136:139], a[88:91], v[164:167]\n"
      "v_exp_f32 v177, v177\n"
      "v_mfma_f32_16x16x32_bf16 v[164:167], a[140:143], a[92:95], v[164:167]\n"
      "v_pk_mul_f32 v[220:221], v[200:201], s[28:29] op_sel:[0,1] op_sel_hi:[1,1]\n"
      "v_pk_mul_f32 v[222:223], v[202:203], s[28:29] op_sel:[0,1] op_sel_hi:[1,1]\n"
      "v_pk_fma_f32 v[32:33], v[32:33], v[200:201], v[220:221]\n"
      "v_pk_fma_f32 v[34:35], v[34:35], v[202:203], v[222:223]\n"
      "v_pk_fma_f32 v[36:37], v[36:37], v[200:201], v[220:221]\n"
      "v_pk_fma_f32 v[38:39], v[38:39], v[202:203], v[222:223]\n"
      "v_pk_fma_f32 v[40:41], v[40:41], v[200:201], v[220:221]\n"
      "v_pk_fma_f32 v[42:43], v[42:43], v[202:203], v[222:223]\n"
      "v_pk_fma_f32 v[44:45], v[44:45], v[200:201], v[220:221]\n"
      "v_pk_fma_f32 v[46:47], v[46:47], v[202:203], v[222:223]\n"
      "v_pk_fma_f32 v[48:49], v[48:49], v[200:201], v[220:221]\n"
      "v_pk_fma_f32 v[50:51], v[50:51], v[202:203], v[222:223]\n"
      "v_pk_fma_f32 v[52:53], v[52:53], v[200:201], v[220:221]\n"
      "v_pk_fma_f32 v[54:55], v[54:55], v[202:203], v[222:223]\n"
      "v_pk_fma_f32 v[56:57], v[56:57], v[200:201], v[220:221]\n"
      "v_pk_fma_f32 v[58:59], v[58:59], v[202:203], v[222:223]\n"
      "v_pk_fma_f32 v[60:61], v[60:61], v[200:201], v[220:221]\n"
      "v_pk_fma_f32 v[62:63], v[62:63], v[202:203], v[222:223]\n"
      "v_mfma_f32_16x16x32_bf16 v[160:163], a[156:159], a[92:95], v[208:211]\n"
      "buffer_store_dwordx4 v[12:15], v250, s[48:51], s64 offen offset:512 sc0 sc1 nt\n"
      "v_cvt_pk_bf16_f32 v220, v32, v40\n"
      "v_cvt_pk_bf16_f32 v221, v48, v56\n"
      "v_cvt_pk_bf16_f32 v222, v36, v44\n"
      "v_mfma_f32_16x16x32_bf16 v[160:163], a[152:155], a[88:91], v[160:163]\n"
      "ds_read_b128 a[132:135], v246 offset:256\n"
      "v_cvt_pk_bf16_f32 v223, v52, v60\n"
      "v_mfma_f32_16x16x32_bf16 v[160:163], a[148:151], a[84:87], v[160:163]\n"
      "ds_write_b128 v251, v[220:223] offset:0\n"
      "v_mfma_f32_16x16x32_bf16 v[160:163], a[144:147], a[80:83], v[160:163]\n"
      "ds_read_b64_tr_b16 a[48:49], v247 offset:512\n"
      "v_cvt_pk_bf16_f32 v32, v33, v41\n"
      "v_cvt_pk_bf16_f32 v33, v49, v57\n"
      "v_mfma_f32_16x16x32_bf16 v[168:171], a[160:163], a[80:83], v[208:211]\n"
      "v_cvt_pk_bf16_f32 v40, v37, v45\n"
      "v_cvt_pk_bf16_f32 v41, v53, v61\n"
      "v_mfma_f32_16x16x32_bf16 v[168:171], a[164:167], a[84:87], v[168:171]\n"
      "ds_write2_b64 v251, v[32:33], v[40:41] offset0:32 offset1:33\n"
      "v_mfma_f32_16x16x32_bf16 v[168:171], a[168:171], a[88:91], v[168:171]\n"
      "buffer_store_dwordx4 v[28:31], v250, s[48:51], s64 offen offset:768 sc0 sc1 nt\n"
      "v_cvt_pk_bf16_f32 v48, v34, v42\n"
      "v_cvt_pk_bf16_f32 v49, v50, v58\n"
      "v_cvt_pk_bf16_f32 v56, v38, v46\n"
      "v_mfma_f32_16x16x32_bf16 v[168:171], a[172:175], a[92:95], v[168:171]\n"
      "ds_read_b128 a[136:139], v246 offset:512\n"
      "v_cvt_pk_bf16_f32 v57, v54, v62\n"
      "v_mfma_f32_16x16x32_bf16 v[172:175], a[188:191], a[92:95], v[208:211]\n"
      ".long 0xBEE500FF\n"
      "PAD4%=:\n"
      "s_setpc_b64 s[94:95]\n"
      "ds_write2_b64 v251, v[48:49], v[56:57] offset0:64 offset1:65\n"
      "v_exp_f32 v189, v189\n"
      "v_cvt_pk_bf16_f32 v60, v35, v43\n"
      "v_mfma_f32_16x16x32_bf16 v[172:175], a[184:187], a[88:91], v[172:175]\n"
      "ds_read_b128 a[140:143], v246 offset:768\n"
      "v_cvt_pk_bf16_f32 v61, v51, v59\n"
      "v_exp_f32 v178, v178\n"
      "v_exp_f32 v179, v179\n"
      "v_exp_f32 v190, v190\n"
      "v_exp_f32 v191, v191\n"
      "v_cvt_pk_bf16_f32 v62, v39, v47\n"
      "v_mfma_f32_16x16x32_bf16 v[172:175], a[180:183], a[84:87], v[172:175]\n"
      "ds_read_b64_tr_b16 v[220:221], v245 offset:0\n"
      "v_cvt_pk_bf16_f32 v63, v55, v63\n"
      "v_cvt_pk_bf16_f32 v182, v176, v177\n"
      "v_cvt_pk_bf16_f32 v183, v178, v179\n"
      "v_cvt_pk_bf16_f32 v186, v188, v189\n"
      "v_cvt_pk_bf16_f32 v187, v190, v191\n"
      "v_mfma_f32_16x16x32_bf16 v[172:175], a[176:179], a[80:83], v[172:175]\n"
      "ds_write_b128 v251, v[60:63] offset:768\n"
      "v_smfmac_f32_16x16x64_bf16 v[224:227], v[240:243], v[180:187], v244\n"
      "ds_read_b64_tr_b16 v[222:223], v245 offset:8\n"
      "ds_read_b64_tr_b16 v[36:37], v245 offset:256\n"
      "v_mfma_f32_16x16x32_bf16 v[64:67], v[180:183], a[0:3], v[64:67]\n"
      "ds_read_b64_tr_b16 v[38:39], v245 offset:264\n"
      "ds_read_b64_tr_b16 v[44:45], v245 offset:512\n"
      "v_mfma_f32_16x16x32_bf16 v[64:67], v[184:187], a[224:227], v[64:67]\n"
      "ds_read_b64_tr_b16 v[46:47], v245 offset:520\n"
      "ds_read_b64_tr_b16 v[60:61], v245 offset:768\n"
      "v_exp_f32 v164, v164\n"
      "v_mfma_f32_16x16x32_bf16 v[68:71], v[184:187], a[228:231], v[68:71]\n"
      "ds_read_b64_tr_b16 v[62:63], v245 offset:776\n"
      "v_exp_f32 v168, v168\n"
      "v_mfma_f32_16x16x32_bf16 v[68:71], v[180:183], a[4:7], v[68:71]\n"
      "ds_read_b128 a[144:147], v246 offset:4096\n"
      "v_cmp_nge_f32 vcc, 0x5E800000, v224\n"
      "v_mfma_f32_16x16x32_bf16 v[72:75], v[180:183], a[8:11], v[72:75]\n"
      "s_or_b64 s[56:57], s[56:57], vcc\n"
      "v_exp_f32 v165, v165\n"
      "v_mfma_f32_16x16x4_f32 v[204:207], v224, v255, 0\n"
      "ds_read_b64_tr_b16 a[50:51], v247 offset:4608\n"
      "v_cvt_pk_bf16_f32 v164, v164, v165\n"
      "ds_read_b128 a[148:151], v246 offset:4352\n"
      "v_cmp_nge_f32 vcc, 0x5E800000, v236\n"
      "v_mfma_f32_16x16x32_bf16 v[72:75], v[184:187], a[232:235], v[72:75]\n"
      "s_or_b64 s[56:57], s[56:57], vcc\n"
      "v_exp_f32 v169, v169\n"
      "v_mfma_f32_16x16x32_bf16 v[76:79], v[184:187], a[236:239], v[76:79]\n"
      "ds_read_b128 a[152:155], v246 offset:4608\n"
      "v_cvt_pk_bf16_f32 v168, v168, v169\n"
      "v_mfma_f32_16x16x32_bf16 v[76:79], v[180:183], a[12:15], v[76:79]\n"
      "s_waitcnt lgkmcnt(9)\n"
      "buffer_store_dwordx4 v[220:223], v250, s[48:51], s65 offen offset:0 sc0 sc1 nt\n"
      "v_rcp_f32 v204, v204\n"
      "v_mfma_f32_16x16x32_bf16 v[80:83], v[180:183], a[16:19], v[80:83]\n"
      "v_rcp_f32 v205, v205\n"
      "v_mfma_f32_16x16x32_bf16 v[80:83], v[184:187], a[240:243], v[80:83]\n"
      "ds_read_b128 a[156:159], v246 offset:4864\n"
      "v_xor_b32 v246, 0x2000, v246\n"
      "v_rcp_f32 v206, v206\n"
      "v_mfma_f32_16x16x32_bf16 v[84:87], v[184:187], a[244:247], v[84:87]\n"
      "v_rcp_f32 v207, v207\n"
      "v_mfma_f32_16x16x32_bf16 v[84:87], v[180:183], a[20:23], v[84:87]\n"
      "v_exp_f32 v166, v166\n"
      "v_mfma_f32_16x16x32_bf16 v[88:91], v[180:183], a[24:27], v[88:91]\n"
      "v_exp_f32 v170, v170\n"
      "v_mfma_f32_16x16x32_bf16 v[88:91], v[184:187], a[248:251], v[88:91]\n"
      "ds_read_b128 a[160:163], v246 offset:0\n"
      "ds_read_b128 a[164:167], v246 offset:256\n"
      "v_mfma_f32_16x16x32_bf16 v[92:95], v[184:187], a[252:255], v[92:95]\n"
      "s_waitcnt lgkmcnt(3)\n"
      "buffer_store_dwordx4 v[36:39], v250, s[48:51], s65 offen offset:256 sc0 sc1 nt\n"
      "v_exp_f32 v167, v167\n"
      "v_mfma_f32_16x16x32_bf16 v[92:95], v[180:183], a[28:31], v[92:95]\n"
      "ds_read_b128 a[168:171], v246 offset:512\n"
      "v_cvt_pk_bf16_f32 v165, v166, v167\n"
      "v_mfma_f32_16x16x32_bf16 v[180:183], a[128:131], a[80:83], v[208:211]\n"
      "ds_read_b128 a[172:175], v246 offset:768\n"
      "ds_read_b128 a[176:179], v246 offset:4096\n"
      "v_mfma_f32_16x16x32_bf16 v[180:183], a[132:135], a[84:87], v[180:183]\n"
      "ds_read_b128 a[188:191], v246 offset:4864\n"
      "v_exp_f32 v171, v171\n"
      "v_mfma_f32_16x16x32_bf16 v[180:183], a[136:139], a[88:91], v[180:183]\n"
      "ds_read_b64_tr_b16 a[52:53], v247 offset:520\n"
      "ds_read_b128 a[184:187], v246 offset:4608\n"
      "v_cvt_pk_bf16_f32 v169, v170, v171\n"
      "v_mfma_f32_16x16x32_bf16 v[180:183], a[140:143], a[92:95], v[180:183]\n"
      "s_waitcnt lgkmcnt(8)\n"
      "ds_read_b128 a[180:183], v246 offset:4352\n"
      "v_xor_b32 v246, 0x6000, v246\n"
      "v_pk_mul_f32 v[224:225], v[204:205], s[28:29] op_sel:[0,1] op_sel_hi:[1,1]\n"
      "v_pk_mul_f32 v[226:227], v[206:207], s[28:29] op_sel:[0,1] op_sel_hi:[1,1]\n"
      "v_pk_fma_f32 v[64:65], v[64:65], v[204:205], v[224:225]\n"
      "v_pk_fma_f32 v[66:67], v[66:67], v[206:207], v[226:227]\n"
      "v_pk_fma_f32 v[68:69], v[68:69], v[204:205], v[224:225]\n"
      "v_pk_fma_f32 v[70:71], v[70:71], v[206:207], v[226:227]\n"
      "v_pk_fma_f32 v[72:73], v[72:73], v[204:205], v[224:225]\n"
      "v_pk_fma_f32 v[74:75], v[74:75], v[206:207], v[226:227]\n"
      "v_pk_fma_f32 v[76:77], v[76:77], v[204:205], v[224:225]\n"
      "v_pk_fma_f32 v[78:79], v[78:79], v[206:207], v[226:227]\n"
      "v_pk_fma_f32 v[80:81], v[80:81], v[204:205], v[224:225]\n"
      "v_pk_fma_f32 v[82:83], v[82:83], v[206:207], v[226:227]\n"
      "v_pk_fma_f32 v[84:85], v[84:85], v[204:205], v[224:225]\n"
      "v_pk_fma_f32 v[86:87], v[86:87], v[206:207], v[226:227]\n"
      "v_pk_fma_f32 v[88:89], v[88:89], v[204:205], v[224:225]\n"
      "v_pk_fma_f32 v[90:91], v[90:91], v[206:207], v[226:227]\n"
      "v_pk_fma_f32 v[92:93], v[92:93], v[204:205], v[224:225]\n"
      "v_pk_fma_f32 v[94:95], v[94:95], v[206:207], v[226:227]\n"
      "v_mfma_f32_16x16x32_bf16 v[176:179], a[156:159], a[92:95], v[208:211]\n"
      "buffer_store_dwordx4 v[44:47], v250, s[48:51], s65 offen offset:512 sc0 sc1 nt\n"
      "v_cvt_pk_bf16_f32 v224, v64, v72\n"
      "v_cvt_pk_bf16_f32 v225, v80, v88\n"
      "v_cvt_pk_bf16_f32 v226, v68, v76\n"
      "v_mfma_f32_16x16x32_bf16 v[176:179], a[152:155], a[88:91], v[176:179]\n"
      "ds_read_b128 a[64:67], v246 offset:0\n"
      "v_cvt_pk_bf16_f32 v227, v84, v92\n"
      "v_mfma_f32_16x16x32_bf16 v[176:179], a[148:151], a[84:87], v[176:179]\n"
      "ds_read_b64_tr_b16 a[54:55], v247 offset:4616\n"
      "ds_write_b128 v251, v[224:227] offset:0\n"
      "v_cvt_pk_bf16_f32 v64, v65, v73\n"
      "v_mfma_f32_16x16x32_bf16 v[176:179], a[144:147], a[80:83], v[176:179]\n"
      "s_waitcnt lgkmcnt(9)\n"
      "ds_read_b128 a[68:71], v246 offset:256\n"
      "v_cvt_pk_bf16_f32 v65, v81, v89\n"
      "v_mfma_f32_16x16x32_bf16 v[184:187], a[160:163], a[80:83], v[208:211]\n"
      "ds_read_b64_tr_b16 a[56:57], v247 offset:768\n"
      "v_cvt_pk_bf16_f32 v72, v69, v77\n"
      "v_cvt_pk_bf16_f32 v73, v85, v93\n"
      "v_mfma_f32_16x16x32_bf16 v[184:187], a[164:167], a[84:87], v[184:187]\n"
      "ds_read_b128 a[72:75], v246 offset:512\n"
      "ds_write2_b64 v251, v[64:65], v[72:73] offset0:32 offset1:33\n"
      "v_cvt_pk_bf16_f32 v80, v66, v74\n"
      "v_mfma_f32_16x16x32_bf16 v[184:187], a[168:171], a[88:91], v[184:187]\n"
      "s_waitcnt lgkmcnt(8)\n"
      "buffer_store_dwordx4 v[60:63], v250, s[48:51], s65 offen offset:768 sc0 sc1 nt\n"
      "v_exp_f32 v160, v160\n"
      "v_cvt_pk_bf16_f32 v81, v82, v90\n"
      "v_mfma_f32_16x16x32_bf16 v[184:187], a[172:175], a[92:95], v[184:187]\n"
      "v_exp_f32 v172, v172\n"
      "v_cvt_pk_bf16_f32 v88, v70, v78\n"
      "v_cvt_pk_bf16_f32 v89, v86, v94\n"
      "v_mfma_f32_16x16x32_bf16 v[188:191], a[188:191], a[92:95], v[208:211]\n"
      "ds_write2_b64 v251, v[80:81], v[88:89] offset0:64 offset1:65\n"
      "v_exp_f32 v161, v161\n"
      "v_cvt_pk_bf16_f32 v92, v67, v75\n"
      "v_mfma_f32_16x16x32_bf16 v[188:191], a[184:187], a[88:91], v[188:191]\n"
      "s_waitcnt lgkmcnt(7)\n"
      "v_exp_f32 v173, v173\n"
      "v_exp_f32 v162, v162\n"
      "v_exp_f32 v163, v163\n"
      "v_exp_f32 v174, v174\n"
      "v_exp_f32 v175, v175\n"
      "v_cvt_pk_bf16_f32 v93, v83, v91\n"
      "v_cvt_pk_bf16_f32 v94, v71, v79\n"
      "v_mfma_f32_16x16x32_bf16 v[188:191], a[180:183], a[84:87], v[188:191]\n"
      "ds_read_b64_tr_b16 v[224:225], v245 offset:0\n"
      "v_cvt_pk_bf16_f32 v95, v87, v95\n"
      "v_cvt_pk_bf16_f32 v166, v160, v161\n"
      "v_cvt_pk_bf16_f32 v167, v162, v163\n"
      "v_cvt_pk_bf16_f32 v170, v172, v173\n"
      "v_cvt_pk_bf16_f32 v171, v174, v175\n"
      "v_mfma_f32_16x16x32_bf16 v[188:191], a[176:179], a[80:83], v[188:191]\n"
      "ds_write_b128 v251, v[92:95] offset:768\n"
      "v_smfmac_f32_16x16x64_bf16 v[228:231], v[240:243], v[164:171], v244\n"
      "ds_read_b64_tr_b16 v[226:227], v245 offset:8\n"
      "ds_read_b64_tr_b16 v[68:69], v245 offset:256\n"
      "v_mfma_f32_16x16x32_bf16 v[96:99], v[164:167], a[0:3], v[96:99]\n"
      "ds_read_b64_tr_b16 v[70:71], v245 offset:264\n"
      "ds_read_b64_tr_b16 v[76:77], v245 offset:512\n"
      "v_mfma_f32_16x16x32_bf16 v[96:99], v[168:171], a[224:227], v[96:99]\n"
      "ds_read_b64_tr_b16 v[78:79], v245 offset:520\n"
      "ds_read_b64_tr_b16 v[92:93], v245 offset:768\n"
      "v_mfma_f32_16x16x32_bf16 v[100:103], v[168:171], a[228:231], v[100:103]\n"
      "ds_read_b64_tr_b16 v[94:95], v245 offset:776\n"
      "ds_read_b64_tr_b16 a[58:59], v247 offset:4864\n"
      "v_mfma_f32_16x16x32_bf16 v[100:103], v[164:167], a[4:7], v[100:103]\n"
      "v_exp_f32 v180, v180\n"
      "v_mfma_f32_16x16x32_bf16 v[104:107], v[164:167], a[8:11], v[104:107]\n"
      "v_exp_f32 v184, v184\n"
      "v_mfma_f32_16x16x32_bf16 v[104:107], v[168:171], a[232:235], v[104:107]\n"
      "v_exp_f32 v181, v181\n"
      "v_mfma_f32_16x16x32_bf16 v[108:111], v[168:171], a[236:239], v[108:111]\n"
      "ds_read_b128 a[76:79], v246 offset:768\n"
      "v_cvt_pk_bf16_f32 v180, v180, v181\n"
      "v_mfma_f32_16x16x32_bf16 v[108:111], v[164:167], a[12:15], v[108:111]\n"
      "v_exp_f32 v185, v185\n"
      "v_mfma_f32_16x16x32_bf16 v[112:115], v[164:167], a[16:19], v[112:115]\n"
      "ds_read_b64_tr_b16 a[60:61], v247 offset:776\n"
      "v_cvt_pk_bf16_f32 v184, v184, v185\n"
      "v_mfma_f32_16x16x32_bf16 v[112:115], v[168:171], a[240:243], v[112:115]\n"
      "s_waitcnt lgkmcnt(4)\n"
      "buffer_store_dwordx4 v[224:227], v250, s[48:51], s66 offen offset:0 sc0 sc1 nt\n"
      "v_exp_f32 v182, v182\n"
      "v_mfma_f32_16x16x32_bf16 v[116:119], v[168:171], a[244:247], v[116:119]\n"
      "v_exp_f32 v186, v186\n"
      "v_mfma_f32_16x16x32_bf16 v[116:119], v[164:167], a[20:23], v[116:119]\n"
      "v_exp_f32 v183, v183\n"
      "v_mfma_f32_16x16x32_bf16 v[120:123], v[164:167], a[24:27], v[120:123]\n"
      "ds_read_b128 a[80:83], v246 offset:4096\n"
      "v_cvt_pk_bf16_f32 v181, v182, v183\n"
      "v_mfma_f32_16x16x32_bf16 v[120:123], v[168:171], a[248:251], v[120:123]\n"
      "v_exp_f32 v187, v187\n"
      "v_mfma_f32_16x16x32_bf16 v[124:127], v[168:171], a[252:255], v[124:127]\n"
      "ds_read_b64_tr_b16 a[62:63], v247 offset:4872\n"
      "v_cvt_pk_bf16_f32 v185, v186, v187\n"
      "v_mfma_f32_16x16x32_bf16 v[124:127], v[164:167], a[28:31], v[124:127]\n"
      "v_exp_f32 v176, v176\n"
      "v_mfma_f32_16x16x32_bf16 v[164:167], a[128:131], a[96:99], v[192:195]\n"
      "v_exp_f32 v188, v188\n"
      "v_mfma_f32_16x16x32_bf16 v[164:167], a[132:135], a[100:103], v[164:167]\n"
      "buffer_store_dwordx4 v[68:71], v250, s[48:51], s66 offen offset:256 sc0 sc1 nt\n"
      "v_exp_f32 v177, v177\n"
      "v_mfma_f32_16x16x32_bf16 v[164:167], a[136:139], a[104:107], v[164:167]\n"
      "ds_read_b128 a[84:87], v246 offset:4352\n"
      "v_cvt_pk_bf16_f32 v182, v176, v177\n"
      "v_mfma_f32_16x16x32_bf16 v[164:167], a[140:143], a[108:111], v[164:167]\n"
      "v_exp_f32 v189, v189\n"
      "v_mfma_f32_16x16x32_bf16 v[160:163], a[156:159], a[108:111], v[192:195]\n"
      "ds_read_b128 v[0:3], v212 offset:0\n"
      "v_cvt_pk_bf16_f32 v186, v188, v189\n"
      "v_mfma_f32_16x16x32_bf16 v[160:163], a[152:155], a[104:107], v[160:163]\n"
      "v_exp_f32 v178, v178\n"
      "v_mfma_f32_16x16x32_bf16 v[160:163], a[148:151], a[100:103], v[160:163]\n"
      "v_exp_f32 v190, v190\n"
      "v_mfma_f32_16x16x32_bf16 v[160:163], a[144:147], a[96:99], v[160:163]\n"
      "v_exp_f32 v179, v179\n"
      "v_mfma_f32_16x16x32_bf16 v[168:171], a[160:163], a[96:99], v[192:195]\n"
      "ds_read_b128 v[8:11], v212 offset:16\n"
      "v_cvt_pk_bf16_f32 v183, v178, v179\n"
      "v_mfma_f32_16x16x32_bf16 v[168:171], a[164:167], a[100:103], v[168:171]\n"
      "buffer_store_dwordx4 v[76:79], v250, s[48:51], s66 offen offset:512 sc0 sc1 nt\n"
      "v_exp_f32 v191, v191\n"
      "v_mfma_f32_16x16x32_bf16 v[168:171], a[168:171], a[104:107], v[168:171]\n"
      "ds_read_b128 v[16:19], v212 offset:8448\n"
      "v_cvt_pk_bf16_f32 v187, v190, v191\n"
      "v_mfma_f32_16x16x32_bf16 v[168:171], a[172:175], a[108:111], v[168:171]\n"
      "v_rcp_f32 v238, v236\n"
      "v_mfma_f32_16x16x32_bf16 v[172:175], a[188:191], a[108:111], v[192:195]\n"
      "v_exp_f32 v164, v164\n"
      "v_mfma_f32_16x16x32_bf16 v[172:175], a[184:187], a[104:107], v[172:175]\n"
      "v_exp_f32 v165, v165\n"
      "v_mfma_f32_16x16x32_bf16 v[172:175], a[180:183], a[100:103], v[172:175]\n"
      "ds_read_b128 a[88:91], v246 offset:4608\n"
      "ds_read_b128 v[24:27], v212 offset:8464\n"
      "v_cvt_pk_bf16_f32 v164, v164, v165\n"
      "v_mfma_f32_16x16x32_bf16 v[172:175], a[176:179], a[96:99], v[172:175]\n"
      "v_exp_f32 v168, v168\n"
      "v_smfmac_f32_16x16x64_bf16 v[228:231], v[240:243], v[180:187], v244\n"
      "ds_read_b128 v[32:35], v213 offset:0\n"
      "v_mfma_f32_16x16x32_bf16 v[96:99], v[180:183], a[192:195], v[96:99]\n"
      "s_waitcnt lgkmcnt(4)\n"
      "buffer_store_dwordx4 v[92:95], v250, s[48:51], s66 offen offset:768 sc0 sc1 nt\n"
      "v_exp_f32 v169, v169\n"
      "v_mfma_f32_16x16x32_bf16 v[96:99], v[184:187], a[32:35], v[96:99]\n"
      "ds_read_b128 v[40:43], v213 offset:16\n"
      "v_cvt_pk_bf16_f32 v168, v168, v169\n"
      "v_mfma_f32_16x16x32_bf16 v[100:103], v[184:187], a[36:39], v[100:103]\n"
      "v_exp_f32 v166, v166\n"
      "v_mfma_f32_16x16x32_bf16 v[100:103], v[180:183], a[196:199], v[100:103]\n"
      "ds_read_b128 v[48:51], v213 offset:8448\n"
      "v_cmp_nge_f32 vcc, 0x5E800000, v228\n"
      "v_mfma_f32_16x16x32_bf16 v[104:107], v[180:183], a[200:203], v[104:107]\n"
      "s_or_b64 s[56:57], s[56:57], vcc\n"
      "v_exp_f32 v170, v170\n"
      "v_mfma_f32_16x16x4_f32 v[208:211], v228, v255, 0\n"
      "ds_read_b128 v[56:59], v213 offset:8464\n"
      "ds_read_b128 a[92:95], v246 offset:4864\n"
      "v_exp_f32 v167, v167\n"
      "v_mfma_f32_16x16x32_bf16 v[104:107], v[184:187], a[40:43], v[104:107]\n"
      "v_xor_b32 v246, 0x2000, v246\n"
      "v_cvt_pk_bf16_f32 v165, v166, v167\n"
      "v_mfma_f32_16x16x32_bf16 v[108:111], v[184:187], a[44:47], v[108:111]\n"
      "v_exp_f32 v171, v171\n"
      "v_mfma_f32_16x16x32_bf16 v[108:111], v[180:183], a[204:207], v[108:111]\n"
      "ds_read_b128 a[160:163], v246 offset:0\n"
      "v_cvt_pk_bf16_f32 v169, v170, v171\n"
      "v_rcp_f32 v208, v208\n"
      "v_mfma_f32_16x16x32_bf16 v[112:115], v[180:183], a[208:211], v[112:115]\n"
      "v_rcp_f32 v209, v209\n"
      "v_mfma_f32_16x16x32_bf16 v[112:115], v[184:187], a[48:51], v[112:115]\n"
      "v_rcp_f32 v210, v210\n"
      "v_mfma_f32_16x16x32_bf16 v[116:119], v[184:187], a[52:55], v[116:119]\n"
      "v_rcp_f32 v211, v211\n"
      "v_mfma_f32_16x16x32_bf16 v[116:119], v[180:183], a[212:215], v[116:119]\n"
      "v_exp_f32 v160, v160\n"
      "v_mfma_f32_16x16x32_bf16 v[120:123], v[180:183], a[216:219], v[120:123]\n"
      "v_exp_f32 v172, v172\n"
      "v_mfma_f32_16x16x32_bf16 v[120:123], v[184:187], a[56:59], v[120:123]\n"
      "s_waitcnt lgkmcnt(1)\n"
      "v_pk_add_f32 v[0:1], v[0:1], v[16:17]\n"
      "v_pk_add_f32 v[2:3], v[2:3], v[18:19]\n"
      "v_pk_add_f32 v[8:9], v[8:9], v[24:25]\n"
      "v_pk_add_f32 v[10:11], v[10:11], v[26:27]\n"
      "v_pk_add_f32 v[32:33], v[32:33], v[48:49]\n"
      "v_pk_add_f32 v[34:35], v[34:35], v[50:51]\n"
      "v_pk_add_f32 v[40:41], v[40:41], v[56:57]\n"
      "v_pk_add_f32 v[42:43], v[42:43], v[58:59]\n"
      "v_pk_add_f32 v[0:1], v[0:1], v[32:33]\n"
      "v_pk_add_f32 v[2:3], v[2:3], v[34:35]\n"
      "v_pk_add_f32 v[8:9], v[8:9], v[40:41]\n"
      "v_pk_add_f32 v[10:11], v[10:11], v[42:43]\n"
      "v_pk_mul_f32 v[0:1], v[0:1], v[238:239] op_sel_hi:[1,0]\n"
      "v_pk_mul_f32 v[2:3], v[2:3], v[238:239] op_sel_hi:[1,0]\n"
      "v_pk_mul_f32 v[8:9], v[8:9], v[238:239] op_sel_hi:[1,0]\n"
      "v_pk_mul_f32 v[10:11], v[10:11], v[238:239] op_sel_hi:[1,0]\n"
      "v_mfma_f32_16x16x32_bf16 v[124:127], v[184:187], a[60:63], v[124:127]\n"
      "ds_read_b128 a[164:167], v246 offset:256\n"
      "v_cvt_pk_bf16_f32 v16, v0, v1\n"
      "v_mfma_f32_16x16x32_bf16 v[124:127], v[180:183], a[220:223], v[124:127]\n"
      "ds_read_b128 a[168:171], v246 offset:512\n"
      "v_cvt_pk_bf16_f32 v17, v2, v3\n"
      "v_mfma_f32_16x16x32_bf16 v[180:183], a[64:67], a[96:99], v[192:195]\n"
      "ds_read_b128 a[172:175], v246 offset:768\n"
      "v_cvt_pk_bf16_f32 v18, v8, v9\n"
      "v_mfma_f32_16x16x32_bf16 v[180:183], a[68:71], a[100:103], v[180:183]\n"
      "ds_read_b128 a[176:179], v246 offset:4096\n"
      "ds_read_b128 a[188:191], v246 offset:4864\n"
      "v_cvt_pk_bf16_f32 v19, v10, v11\n"
      "v_mfma_f32_16x16x32_bf16 v[180:183], a[72:75], a[104:107], v[180:183]\n"
      "buffer_store_dwordx4 v[16:19], v215, s[48:51], s69 offen offset:0 sc0 sc1 nt\n"
      "ds_read_b128 a[184:187], v246 offset:4608\n"
      "v_exp_f32 v161, v161\n"
      "v_mfma_f32_16x16x32_bf16 v[180:183], a[76:79], a[108:111], v[180:183]\n"
      "ds_read_b128 a[180:183], v246 offset:4352\n"
      "v_cvt_pk_bf16_f32 v166, v160, v161\n"
      "v_pk_mul_f32 v[228:229], v[208:209], s[28:29] op_sel:[0,1] op_sel_hi:[1,1]\n"
      "v_pk_mul_f32 v[230:231], v[210:211], s[28:29] op_sel:[0,1] op_sel_hi:[1,1]\n"
      "v_pk_fma_f32 v[96:97], v[96:97], v[208:209], v[228:229]\n"
      "v_pk_fma_f32 v[98:99], v[98:99], v[210:211], v[230:231]\n"
      "v_pk_fma_f32 v[100:101], v[100:101], v[208:209], v[228:229]\n"
      "v_pk_fma_f32 v[102:103], v[102:103], v[210:211], v[230:231]\n"
      "v_pk_fma_f32 v[104:105], v[104:105], v[208:209], v[228:229]\n"
      "v_pk_fma_f32 v[106:107], v[106:107], v[210:211], v[230:231]\n"
      "v_pk_fma_f32 v[108:109], v[108:109], v[208:209], v[228:229]\n"
      "v_pk_fma_f32 v[110:111], v[110:111], v[210:211], v[230:231]\n"
      "v_pk_fma_f32 v[112:113], v[112:113], v[208:209], v[228:229]\n"
      "v_pk_fma_f32 v[114:115], v[114:115], v[210:211], v[230:231]\n"
      "v_pk_fma_f32 v[116:117], v[116:117], v[208:209], v[228:229]\n"
      "v_pk_fma_f32 v[118:119], v[118:119], v[210:211], v[230:231]\n"
      "v_pk_fma_f32 v[120:121], v[120:121], v[208:209], v[228:229]\n"
      "v_pk_fma_f32 v[122:123], v[122:123], v[210:211], v[230:231]\n"
      "v_pk_fma_f32 v[124:125], v[124:125], v[208:209], v[228:229]\n"
      "v_pk_fma_f32 v[126:127], v[126:127], v[210:211], v[230:231]\n"
      "v_mfma_f32_16x16x32_bf16 v[176:179], a[92:95], a[108:111], v[192:195]\n"
      "v_cvt_pk_bf16_f32 v228, v96, v104\n"
      "v_cvt_pk_bf16_f32 v229, v112, v120\n"
      "v_mfma_f32_16x16x32_bf16 v[176:179], a[88:91], a[104:107], v[176:179]\n"
      "v_cvt_pk_bf16_f32 v230, v100, v108\n"
      "v_cvt_pk_bf16_f32 v231, v116, v124\n"
      "v_mfma_f32_16x16x32_bf16 v[176:179], a[84:87], a[100:103], v[176:179]\n"
      "ds_write_b128 v251, v[228:231] offset:0\n"
      "v_cvt_pk_bf16_f32 v96, v97, v105\n"
      "v_cvt_pk_bf16_f32 v97, v113, v121\n"
      "v_mfma_f32_16x16x32_bf16 v[176:179], a[80:83], a[96:99], v[176:179]\n"
      "s_waitcnt lgkmcnt(6)\n"
      "v_cvt_pk_bf16_f32 v104, v101, v109\n"
      "v_cvt_pk_bf16_f32 v105, v117, v125\n"
      "v_mfma_f32_16x16x32_bf16 v[184:187], a[160:163], a[96:99], v[192:195]\n"
      "ds_write2_b64 v251, v[96:97], v[104:105] offset0:32 offset1:33\n"
      "v_cvt_pk_bf16_f32 v112, v98, v106\n"
      "v_cvt_pk_bf16_f32 v113, v114, v122\n"
      "v_mfma_f32_16x16x32_bf16 v[184:187], a[164:167], a[100:103], v[184:187]\n"
      "v_cvt_pk_bf16_f32 v120, v102, v110\n"
      "v_cvt_pk_bf16_f32 v121, v118, v126\n"
      "v_mfma_f32_16x16x32_bf16 v[184:187], a[168:171], a[104:107], v[184:187]\n"
      "s_waitcnt lgkmcnt(3)\n"
      "ds_write2_b64 v251, v[112:113], v[120:121] offset0:64 offset1:65\n"
      "v_cvt_pk_bf16_f32 v124, v99, v107\n"
      "v_mfma_f32_16x16x32_bf16 v[184:187], a[172:175], a[108:111], v[184:187]\n"
      "v_cvt_pk_bf16_f32 v125, v115, v123\n"
      "v_cvt_pk_bf16_f32 v126, v103, v111\n"
      "v_cvt_pk_bf16_f32 v127, v119, v127\n"
      "v_mfma_f32_16x16x32_bf16 v[188:191], a[188:191], a[108:111], v[192:195]\n"
      "ds_write_b128 v251, v[124:127] offset:768\n"
      "ds_read_b64_tr_b16 v[228:229], v245 offset:0\n"
      "v_exp_f32 v173, v173\n"
      "v_mfma_f32_16x16x32_bf16 v[188:191], a[184:187], a[104:107], v[188:191]\n"
      "s_waitcnt lgkmcnt(5)\n"
      "ds_read_b64_tr_b16 v[230:231], v245 offset:8\n"
      "ds_read_b64_tr_b16 v[100:101], v245 offset:256\n"
      "ds_read_b64_tr_b16 v[102:103], v245 offset:264\n"
      "v_cvt_pk_bf16_f32 v170, v172, v173\n"
      "v_exp_f32 v162, v162\n"
      "v_exp_f32 v163, v163\n"
      "v_exp_f32 v174, v174\n"
      "v_exp_f32 v175, v175\n"
      "v_mfma_f32_16x16x32_bf16 v[188:191], a[180:183], a[100:103], v[188:191]\n"
      "ds_read_b64_tr_b16 v[108:109], v245 offset:512\n"
      "ds_read_b64_tr_b16 v[110:111], v245 offset:520\n"
      "v_cvt_pk_bf16_f32 v167, v162, v163\n"
      "v_cvt_pk_bf16_f32 v171, v174, v175\n"
      "v_mfma_f32_16x16x32_bf16 v[188:191], a[176:179], a[96:99], v[188:191]\n"
      "ds_read_b64_tr_b16 v[124:125], v245 offset:768\n"
      "ds_read_b64_tr_b16 v[126:127], v245 offset:776\n"
      "v_smfmac_f32_16x16x64_bf16 v[232:235], v[240:243], v[164:171], v244\n"
      "v_mfma_f32_16x16x32_bf16 v[128:131], v[164:167], a[192:195], v[128:131]\n"
      "v_exp_f32 v180, v180\n"
      "v_mfma_f32_16x16x32_bf16 v[128:131], v[168:171], a[32:35], v[128:131]\n"
      "v_exp_f32 v184, v184\n"
      "v_mfma_f32_16x16x32_bf16 v[132:135], v[168:171], a[36:39], v[132:135]\n"
      "v_exp_f32 v181, v181\n"
      "v_mfma_f32_16x16x32_bf16 v[132:135], v[164:167], a[196:199], v[132:135]\n"
      "v_exp_f32 v185, v185\n"
      "v_mfma_f32_16x16x32_bf16 v[136:139], v[164:167], a[200:203], v[136:139]\n"
      "v_exp_f32 v182, v182\n"
      "v_mfma_f32_16x16x32_bf16 v[136:139], v[168:171], a[40:43], v[136:139]\n"
      "v_exp_f32 v186, v186\n"
      "v_mfma_f32_16x16x32_bf16 v[140:143], v[168:171], a[44:47], v[140:143]\n"
      "s_waitcnt lgkmcnt(0)\n"
      "buffer_store_dwordx4 v[228:231], v250, s[48:51], s67 offen offset:0 sc0 sc1 nt\n"
      "v_exp_f32 v183, v183\n"
      "v_cvt_pk_bf16_f32 v180, v180, v181\n"
      "v_mfma_f32_16x16x32_bf16 v[140:143], v[164:167], a[204:207], v[140:143]\n"
      "v_exp_f32 v187, v187\n"
      "v_mfma_f32_16x16x32_bf16 v[144:147], v[164:167], a[208:211], v[144:147]\n"
      "v_exp_f32 v176, v176\n"
      "v_mfma_f32_16x16x32_bf16 v[144:147], v[168:171], a[48:51], v[144:147]\n"
      "v_exp_f32 v188, v188\n"
      "v_mfma_f32_16x16x32_bf16 v[148:151], v[168:171], a[52:55], v[148:151]\n"
      "v_exp_f32 v177, v177\n"
      "v_mfma_f32_16x16x32_bf16 v[148:151], v[164:167], a[212:215], v[148:151]\n"
      "v_exp_f32 v189, v189\n"
      "v_mfma_f32_16x16x32_bf16 v[152:155], v[164:167], a[216:219], v[152:155]\n"
      "v_exp_f32 v178, v178\n"
      "v_mfma_f32_16x16x32_bf16 v[152:155], v[168:171], a[56:59], v[152:155]\n"
      "v_exp_f32 v190, v190\n"
      "v_exp_f32 v179, v179\n"
      "v_exp_f32 v191, v191\n"
      "v_mfma_f32_16x16x32_bf16 v[156:159], v[168:171], a[60:63], v[156:159]\n"
      "buffer_store_dwordx4 v[100:103], v250, s[48:51], s67 offen offset:256 sc0 sc1 nt\n"
      "v_cvt_pk_bf16_f32 v181, v182, v183\n"
      "v_cvt_pk_bf16_f32 v182, v176, v177\n"
      "v_cvt_pk_bf16_f32 v183, v178, v179\n"
      "v_cvt_pk_bf16_f32 v184, v184, v185\n"
      "v_cvt_pk_bf16_f32 v185, v186, v187\n"
      "v_cvt_pk_bf16_f32 v186, v188, v189\n"
      "v_cvt_pk_bf16_f32 v187, v190, v191\n"
      "v_mfma_f32_16x16x32_bf16 v[156:159], v[164:167], a[220:223], v[156:159]\n"
      "v_smfmac_f32_16x16x64_bf16 v[232:235], v[240:243], v[180:187], v244\n"
      "v_mfma_f32_16x16x32_bf16 v[128:131], v[180:183], a[0:3], v[128:131]\n"
      "v_mfma_f32_16x16x32_bf16 v[128:131], v[184:187], a[224:227], v[128:131]\n"
      "v_mfma_f32_16x16x32_bf16 v[132:135], v[184:187], a[228:231], v[132:135]\n"
      "v_mfma_f32_16x16x32_bf16 v[132:135], v[180:183], a[4:7], v[132:135]\n"
      "v_cmp_nge_f32 vcc, 0x5E800000, v232\n"
      "v_mfma_f32_16x16x32_bf16 v[136:139], v[180:183], a[8:11], v[136:139]\n"
      "s_or_b64 s[56:57], s[56:57], vcc\n"
      "buffer_store_dwordx4 v[108:111], v250, s[48:51], s67 offen offset:512 sc0 sc1 nt\n"
      "v_mfma_f32_16x16x4_f32 v[192:195], v232, v255, 0\n"
      "v_mfma_f32_16x16x32_bf16 v[136:139], v[184:187], a[232:235], v[136:139]\n"
      "v_mfma_f32_16x16x32_bf16 v[140:143], v[184:187], a[236:239], v[140:143]\n"
      "v_mfma_f32_16x16x32_bf16 v[140:143], v[180:183], a[12:15], v[140:143]\n"
      "s_nop 5\n"
      "v_rcp_f32 v192, v192\n"
      "v_mfma_f32_16x16x32_bf16 v[144:147], v[180:183], a[16:19], v[144:147]\n"
      "v_rcp_f32 v193, v193\n"
      "v_mul_f32 v196, s29, v192\n"
      "v_mfma_f32_16x16x32_bf16 v[144:147], v[184:187], a[240:243], v[144:147]\n"
      "v_rcp_f32 v194, v194\n"
      "v_mul_f32 v197, s29, v193\n"
      "v_mfma_f32_16x16x32_bf16 v[148:151], v[184:187], a[244:247], v[148:151]\n"
      "buffer_store_dwordx4 v[124:127], v250, s[48:51], s67 offen offset:768 sc0 sc1 nt\n"
      "v_rcp_f32 v195, v195\n"
      "v_mul_f32 v198, s29, v194\n"
      "v_mfma_f32_16x16x32_bf16 v[148:151], v[180:183], a[20:23], v[148:151]\n"
      "v_mul_f32 v199, s29, v195\n"
      "v_pk_fma_f32 v[128:129], v[128:129], v[192:193], v[196:197]\n"
      "v_pk_fma_f32 v[132:133], v[132:133], v[192:193], v[196:197]\n"
      "v_pk_fma_f32 v[136:137], v[136:137], v[192:193], v[196:197]\n"
      "v_pk_fma_f32 v[140:141], v[140:141], v[192:193], v[196:197]\n"
      "v_mfma_f32_16x16x32_bf16 v[152:155], v[180:183], a[24:27], v[152:155]\n"
      "v_cvt_pk_bf16_f32 v232, v128, v136\n"
      "v_cvt_pk_bf16_f32 v234, v132, v140\n"
      "v_cvt_pk_bf16_f32 v128, v129, v137\n"
      "v_cvt_pk_bf16_f32 v136, v133, v141\n"
      "v_mfma_f32_16x16x32_bf16 v[152:155], v[184:187], a[248:251], v[152:155]\n"
      "v_mfma_f32_16x16x32_bf16 v[156:159], v[184:187], a[252:255], v[156:159]\n"
      "v_mfma_f32_16x16x32_bf16 v[156:159], v[180:183], a[28:31], v[156:159]\n"
      "v_pk_fma_f32 v[144:145], v[144:145], v[192:193], v[196:197]\n"
      "v_pk_fma_f32 v[148:149], v[148:149], v[192:193], v[196:197]\n"
      "s_nop 1\n"
      "v_pk_fma_f32 v[152:153], v[152:153], v[192:193], v[196:197]\n"
      "s_nop 2\n"
      "v_pk_fma_f32 v[156:157], v[156:157], v[192:193], v[196:197]\n"
      "v_cvt_pk_bf16_f32 v233, v144, v152\n"
      "v_cvt_pk_bf16_f32 v235, v148, v156\n"
      "ds_write_b128 v251, v[232:235] offset:0\n"
      "v_cvt_pk_bf16_f32 v129, v145, v153\n"
      "v_cvt_pk_bf16_f32 v137, v149, v157\n"
      "ds_write2_b64 v251, v[128:129], v[136:137] offset0:32 offset1:33\n"
      "ds_read_b64_tr_b16 v[64:65], v245 offset:0\n"
      "ds_read_b64_tr_b16 v[66:67], v245 offset:8\n"
      "ds_read_b64_tr_b16 v[72:73], v245 offset:256\n"
      "ds_read_b64_tr_b16 v[74:75], v245 offset:264\n"
      "v_pk_fma_f32 v[130:131], v[130:131], v[194:195], v[198:199]\n"
      "v_pk_fma_f32 v[134:135], v[134:135], v[194:195], v[198:199]\n"
      "v_pk_fma_f32 v[138:139], v[138:139], v[194:195], v[198:199]\n"
      "v_pk_fma_f32 v[142:143], v[142:143], v[194:195], v[198:199]\n"
      "v_pk_fma_f32 v[146:147], v[146:147], v[194:195], v[198:199]\n"
      "v_pk_fma_f32 v[150:151], v[150:151], v[194:195], v[198:199]\n"
      "v_pk_fma_f32 v[154:155], v[154:155], v[194:195], v[198:199]\n"
      "v_pk_fma_f32 v[158:159], v[158:159], v[194:195], v[198:199]\n"
      "v_cvt_pk_bf16_f32 v144, v130, v138\n"
      "v_cvt_pk_bf16_f32 v145, v146, v154\n"
      "v_cvt_pk_bf16_f32 v152, v134, v142\n"
      "v_cvt_pk_bf16_f32 v153, v150, v158\n"
      "ds_write2_b64 v251, v[144:145], v[152:153] offset0:64 offset1:65\n"
      "s_waitcnt lgkmcnt(3)\n"
      "buffer_store_dwordx4 v[64:67], v250, s[48:51], s68 offen offset:0 sc0 sc1 nt\n"
      "v_cvt_pk_bf16_f32 v156, v131, v139\n"
      "v_cvt_pk_bf16_f32 v157, v147, v155\n"
      "v_cvt_pk_bf16_f32 v158, v135, v143\n"
      "v_cvt_pk_bf16_f32 v159, v151, v159\n"
      "ds_write_b128 v251, v[156:159] offset:768\n"
      "ds_read_b64_tr_b16 v[80:81], v245 offset:512\n"
      "ds_read_b64_tr_b16 v[82:83], v245 offset:520\n"
      "ds_read_b64_tr_b16 v[88:89], v245 offset:768\n"
      "ds_read_b64_tr_b16 v[90:91], v245 offset:776\n"
      "s_waitcnt lgkmcnt(6)\n"
      "buffer_store_dwordx4 v[72:75], v250, s[48:51], s68 offen offset:256 sc0 sc1 nt\n"
      "s_waitcnt lgkmcnt(2)\n"
      "buffer_store_dwordx4 v[80:83], v250, s[48:51], s68 offen offset:512 sc0 sc1 nt\n"
      "s_waitcnt lgkmcnt(0)\n"
      "buffer_store_dwordx4 v[88:91], v250, s[48:51], s68 offen offset:768 sc0 sc1 nt\n"
      "s_or_b32 %[flag], s56, s57\n"
      "s_cbranch_scc1 SLOW%=\n"
      "s_endpgm\n"
      "SLOW%=:\n"
      "s_nop 1\n"
      : [flag] "=&s"(flag)
      : [ka] "s"(__builtin_amdgcn_kernarg_segment_ptr()), [bid] "s"(bid), [w] "s"(w), [lds] "s"((unsigned)(size_t)lds)
      : "v0","v1","v2","v3","v4","v5","v6","v7","v8","v9","v10","v11","v12","v13","v14","v15","v16","v17","v18","v19","v20","v21","v22","v23","v24","v25","v26","v27","v28","v29","v30","v31","v32","v33","v34","v35","v36","v37","v38","v39","v40","v41","v42","v43","v44","v45","v46","v47","v48","v49","v50","v51","v52","v53","v54","v55","v56","v57","v58","v59","v60","v61","v62","v63","v64","v65","v66","v67","v68","v69","v70","v71","v72","v73","v74","v75","v76","v77","v78","v79","v80","v81","v82","v83","v84","v85","v86","v87","v88","v89","v90","v91","v92","v93","v94","v95","v96","v97","v98","v99","v100","v101","v102","v103","v104","v105","v106","v107","v108","v109","v110","v111","v112","v113","v114","v115","v116","v117","v118","v119","v120","v121","v122","v123","v124","v125","v126","v127","v128","v129","v130","v131","v132","v133","v134","v135","v136","v137","v138","v139","v140","v141","v142","v143","v144","v145","v146","v147","v148","v149","v150","v151","v152","v153","v154","v155","v156","v157","v158","v159","v160","v161","v162","v163","v164","v165","v166","v167","v168","v169","v170","v171","v172","v173","v174","v175","v176","v177","v178","v179","v180","v181","v182","v183","v184","v185","v186","v187","v188","v189","v190","v191","v192","v193","v194","v195","v196","v197","v198","v199","v200","v201","v202","v203","v204","v205","v206","v207","v208","v209","v210","v211","v212","v213","v214","v215","v216","v217","v218","v219","v220","v221","v222","v223","v224","v225","v226","v227","v228","v229","v230","v231","v232","v233","v234","v235","v236","v237","v238","v239","v240","v241","v242","v243","v244","v245","v246","v247","v248","v249","v250","v251","v252","v253","v254","v255","a0","a1","a2","a3","a4","a5","a6","a7","a8","a9","a10","a11","a12","a13","a14","a15","a16","a17","a18","a19","a20","a21","a22","a23","a24","a25","a26","a27","a28","a29","a30","a31","a32","a33","a34","a35","a36","a37","a38","a39","a40","a41","a42","a43","a44","a45","a46","a47","a48","a49","a50","a51","a52","a53","a54","a55","a56","a57","a58","a59","a60","a61","a62","a63","a64","a65","a66","a67","a68","a69","a70","a71","a72","a73","a74","a75","a76","a77","a78","a79","a80","a81","a82","a83","a84","a85","a86","a87","a88","a89","a90","a91","a92","a93","a94","a95","a96","a97","a98","a99","a100","a101","a102","a103","a104","a105","a106","a107","a108","a109","a110","a111","a112","a113","a114","a115","a116","a117","a118","a119","a120","a121","a122","a123","a124","a125","a126","a127","a128","a129","a130","a131","a132","a133","a134","a135","a136","a137","a138","a139","a140","a141","a142","a143","a144","a145","a146","a147","a148","a149","a150","a151","a152","a153","a154","a155","a156","a157","a158","a159","a160","a161","a162","a163","a164","a165","a166","a167","a168","a169","a170","a171","a172","a173","a174","a175","a176","a177","a178","a179","a180","a181","a182","a183","a184","a185","a186","a187","a188","a189","a190","a191","a192","a193","a194","a195","a196","a197","a198","a199","a200","a201","a202","a203","a204","a205","a206","a207","a208","a209","a210","a211","a212","a213","a214","a215","a216","a217","a218","a219","a220","a221","a222","a223","a224","a225","a226","a227","a228","a229","a230","a231","a232","a233","a234","a235","a236","a237","a238","a239","a240","a241","a242","a243","a244","a245","a246","a247","a248","a249","a250","a251","a252","a253","a254","a255","s28","s29","s30","s31","s32","s33","s34","s35","s36","s37","s38","s39","s40","s41","s42","s43","s44","s45","s46","s47","s48","s49","s50","s51","s52","s53","s54","s55","s56","s57","s58","s59","s60","s61","s62","s63","s64","s65","s66","s67","s68","s69","s70","s71","s72","s73","s74","s75","s76","s77","s78","s79","s80","s81","s82","s83","s84","s85","s86","s87","s88","s89","s90","s91","s92","s93","s94","s95","s96","s97","s98","s99","s100","s101","vcc","scc","m0","memory");
  if (flag) {
    asm volatile("s_waitcnt vmcnt(0)" ::: "memory");
    const unsigned short* q = Q + (size_t)head * SQ * DH;
    const unsigned short* k = K + (size_t)grp * SK * DH;
    const unsigned short* v = V + (size_t)grp * SK * DH;
    unsigned short* o = O + (size_t)head * SQ * DH;
    const int lane = __builtin_amdgcn_mbcnt_hi(~0u, __builtin_amdgcn_mbcnt_lo(~0u, 0u));
    for (int i = 0; i < 84; ++i) {
      const int row = i < 80 ? 80 * (int)w + i : 320 + 4 * (int)w + (i - 80);
      slow_row(q, k, v, o, row, lane);
    }
  }
}

extern "C" void kernel_launch(void* const* d_in, const int* in_sizes, int n_in, void* d_out, int out_size, void* d_ws, size_t ws_size,
                              hipStream_t stream) {
  hipLaunchKernelGGL(gqa_attn_kernel, dim3(256), dim3(256), 0, stream, (const unsigned short*)d_in[0], (const unsigned short*)d_in[1],
                     (const unsigned short*)d_in[2], (unsigned short*)d_out
  );
}
```
